```python
import torch

def _softmax(x, axis=-1):
    return torch.softmax(x, dim=axis)
def _gelu(x):
    return torch.nn.functional.gelu(x, approximate="tanh")
def _silu(x):
    return torch.nn.functional.silu(x)
def _sigmoid(x):
    return torch.sigmoid(x)
def _layernorm(x, axis=-1, eps=1e-5):
    m = x.mean(dim=axis, keepdim=True)
    v = x.var(dim=axis, keepdim=True, unbiased=False)
    return (x - m) / torch.sqrt(v + eps)
def _scatter_add(tgt, idx, src, axis=0):
    return tgt.clone().index_add_(axis, idx, src)
def _segment_sum(src, seg, n):
    out = torch.zeros((n,) + src.shape[1:], dtype=src.dtype)
    return out.index_add_(0, seg, src)
def _pad(x, pw):
    flat = tuple(v for pair in reversed(pw) for v in pair)
    return torch.nn.functional.pad(x, flat)

kernel_name = 'standalone__mha_block_cached__near_ridge'

def setup_inputs():

    _w = (lambda a: (lambda t: t.to(torch.bfloat16) if t.is_floating_point() else t)(torch.as_tensor(a)))
    B, Sq, D = 8, 128, 1024
    H, Sk, Dh = 32, 1024, 128
    return {
        "x": _w((lambda *s: torch.randn(*s, dtype=torch.bfloat16) * 0.8)(B, Sq, D)),
        "k_cache": _w((lambda *s: torch.randn(*s, dtype=torch.bfloat16) * 0.8)(B, H, Sk, Dh)),
        "v_cache": _w((lambda *s: torch.randn(*s, dtype=torch.bfloat16) * 0.05)(B, H, Sk, Dh)),
        "wq": _w((lambda *s: torch.randn(*s, dtype=torch.bfloat16) * 0.8)(D, H * Dh) * (D ** -0.5)),
        "wo": _w((lambda *s: torch.randn(*s, dtype=torch.bfloat16) * 0.05)(H * Dh, D) * ((H * Dh) ** -0.5)),
    }

def reference(x, k_cache, v_cache, wq, wo):
    B, Sq, D = x.shape
    H, Dh = 32, 128
    q = (lambda x, *d: x.permute(*d))((x @ wq).reshape(B, Sq, H, Dh), 0, 2, 1, 3)
    scores = torch.einsum("bhqd,bhkd->bhqk", q, k_cache) * (Dh ** -0.5)
    p = _softmax(scores, axis=-1)
    o = torch.einsum("bhqk,bhkd->bhqd", p, v_cache)
    return (lambda x, *d: x.permute(*d))(o, 0, 2, 1, 3).reshape(B, Sq, H * Dh) @ wo
```

```cpp
#include <hip/hip_runtime.h>
#include <hip/hip_bf16.h>
typedef __hip_bfloat16 bf16;
constexpr int B=8,SQ=128,D=1024,H=32,SK=1024,DH=128;

__global__ void gemm_naive(const bf16* A, const bf16* Bm, bf16* C, int M, int N, int K) {
  __shared__ float As[16][17], Bs[16][17];
  int tx = threadIdx.x, ty = threadIdx.y;
  int row = blockIdx.y*16+ty, col = blockIdx.x*16+tx;
  float acc = 0;
  for (int k0=0;k0<K;k0+=16) {
    As[ty][tx] = __bfloat162float(A[(size_t)row*K + k0+tx]);
    Bs[ty][tx] = __bfloat162float(Bm[(size_t)(k0+ty)*N + col]);
    __syncthreads();
    for (int k=0;k<16;++k) acc += As[ty][k]*Bs[k][tx];
    __syncthreads();
  }
  C[(size_t)row*N+col] = __float2bfloat16(acc);
}
__global__ void attn_naive(const bf16* Q, const bf16* Kc, const bf16* Vc, bf16* O) {
  int q = blockIdx.x, h = blockIdx.y, b = blockIdx.z;
  __shared__ float qs[DH]; __shared__ float s[SK]; __shared__ float red[128];
  int t = threadIdx.x;
  qs[t] = __bfloat162float(Q[((size_t)(b*SQ+q))*(H*DH) + h*DH + t]);
  __syncthreads();
  const bf16* Kb = Kc + ((size_t)(b*H+h))*SK*DH; const bf16* Vb = Vc + ((size_t)(b*H+h))*SK*DH;
  float mx = -1e30f;
  for (int k=t;k<SK;k+=128) { float a=0; for (int d=0;d<DH;++d) a += qs[d]*__bfloat162float(Kb[(size_t)k*DH+d]); a *= 0.08838834764831845f; s[k]=a; mx = fmaxf(mx,a);}
  red[t]=mx; __syncthreads();
  for (int o=64;o>0;o>>=1){ if(t<o) red[t]=fmaxf(red[t],red[t+o]); __syncthreads(); }
  mx = red[0]; __syncthreads();
  float sum=0; for (int k=t;k<SK;k+=128){ float e=__expf(s[k]-mx); s[k]=e; sum+=e; }
  red[t]=sum; __syncthreads();
  for (int o=64;o>0;o>>=1){ if(t<o) red[t]+=red[t+o]; __syncthreads(); }
  float inv = 1.f/red[0];
  float acc=0; for (int k=0;k<SK;++k) acc += s[k]*__bfloat162float(Vb[(size_t)k*DH+t]);
  O[((size_t)(b*SQ+q))*(H*DH) + h*DH + t] = __float2bfloat16(acc*inv);
}
extern "C" void kernel_launch(void* const* d_in, const int* in_sizes, int n_in, void* d_out, int out_size, void* d_ws, size_t ws_size, hipStream_t stream) {
  const bf16* x=(const bf16*)d_in[0]; const bf16* kc=(const bf16*)d_in[1]; const bf16* vc=(const bf16*)d_in[2];
  const bf16* wq=(const bf16*)d_in[3]; const bf16* wo=(const bf16*)d_in[4];
  bf16* q=(bf16*)d_ws; bf16* o = q + (size_t)B*SQ*H*DH;
  gemm_naive<<<dim3(H*DH/16, B*SQ/16), dim3(16,16), 0, stream>>>(x, wq, q, B*SQ, H*DH, D);
  attn_naive<<<dim3(SQ,H,B), 128, 0, stream>>>(q, kc, vc, o);
  gemm_naive<<<dim3(D/16, B*SQ/16), dim3(16,16), 0, stream>>>(o, wo, (bf16*)d_out, B*SQ, D, H*DH);
}
```

```cpp
#include <hip/hip_runtime.h>
#include <hip/hip_bf16.h>

#define KV_NT 1
#define W_NT 0

typedef __bf16 bf16x8 __attribute__((ext_vector_type(8)));
typedef __bf16 bf16x2 __attribute__((ext_vector_type(2)));
typedef short s16x4 __attribute__((ext_vector_type(4)));
typedef short s16x8 __attribute__((ext_vector_type(8)));
typedef float f32x16 __attribute__((ext_vector_type(16)));
typedef float f32x4 __attribute__((ext_vector_type(4)));
typedef float f32x2 __attribute__((ext_vector_type(2)));
typedef unsigned u32x4 __attribute__((ext_vector_type(4)));
typedef unsigned u32x2 __attribute__((ext_vector_type(2)));
#define LAS __attribute__((address_space(3)))
#define DI __device__ __forceinline__
#define MFMA(a, b, c) __builtin_amdgcn_mfma_f32_32x32x16_bf16((a), (b), (c), 0, 0, 0)

constexpr int NB = 8, SQ = 128, DM = 1024, NH = 32, SK = 1024, DH = 128;
constexpr int SLOT = 32768, LDS_BYTES = 163840;
constexpr float C2 = 0.08838834764831845f * 1.4426950408889634f;
constexpr float THR = 8.0f;

DI unsigned rfl(unsigned x) { return __builtin_amdgcn_readfirstlane(x); }
DI unsigned cvtpk(float lo, float hi) { f32x2 v = {lo, hi}; bf16x2 b = __builtin_convertvector(v, bf16x2); return __builtin_bit_cast(unsigned, b); }
DI bf16x8 pack8(const f32x16& x, int s) {
  u32x4 w = {cvtpk(x[8 * s + 0], x[8 * s + 1]), cvtpk(x[8 * s + 2], x[8 * s + 3]), cvtpk(x[8 * s + 4], x[8 * s + 5]), cvtpk(x[8 * s + 6], x[8 * s + 7])};
  return __builtin_bit_cast(bf16x8, w);
}
template <int NT> DI void dma16(__amdgpu_buffer_rsrc_t rs, unsigned voff, unsigned soff, unsigned ldsaddr) {
  if constexpr (NT) asm volatile("s_mov_b32 m0, %0\n\ts_nop 0\n\tbuffer_load_dwordx4 %1, %2, %3 offen nt lds" ::"s"(ldsaddr), "v"(voff), "s"(rs), "s"(soff) : "memory");
  else              asm volatile("s_mov_b32 m0, %0\n\ts_nop 0\n\tbuffer_load_dwordx4 %1, %2, %3 offen lds" ::"s"(ldsaddr), "v"(voff), "s"(rs), "s"(soff) : "memory");
}
DI bf16x8 rd128(const LAS char* p) { return *(const LAS bf16x8*)p; }
DI bf16x8 rdtr(const LAS char* lo, const LAS char* up) {
  s16x4 a = __builtin_amdgcn_ds_read_tr16_b64_v4i16((LAS s16x4*)lo);
  s16x4 b = __builtin_amdgcn_ds_read_tr16_b64_v4i16((LAS s16x4*)up);
  s16x8 c = __builtin_shufflevector(a, b, 0, 1, 2, 3, 4, 5, 6, 7);
  return __builtin_bit_cast(bf16x8, c);
}
#define BAR_L() asm volatile("s_waitcnt lgkmcnt(0)\n\ts_barrier" ::: "memory")
#define WAITV_BAR(N) asm volatile("s_waitcnt vmcnt(" #N ")\n\ts_barrier" ::: "memory")
DI float swap_other(float x) {
  auto r = __builtin_amdgcn_permlane32_swap(__float_as_uint(x), __float_as_uint(x), false, false);
  return (threadIdx.x & 32) ? __uint_as_float(r[0]) : __uint_as_float(r[1]);
}
__host__ __device__ constexpr int kvslot(int t) { return (t % 5) == 0 ? 3 : (t % 5) == 1 ? 4 : (t % 5) == 2 ? 1 : (t % 5) == 3 ? 2 : 0; }

__global__ __launch_bounds__(512, 2) void attn_kernel(const char* __restrict__ x, const char* __restrict__ kc, const char* __restrict__ vc,
                                                      const char* __restrict__ wq, char* __restrict__ ows) {
  __shared__ __attribute__((aligned(16))) char lds[LDS_BYTES];
  const int tid = threadIdx.x, lane = tid & 63;
  const int wid = rfl(tid >> 6);
  const int wg = blockIdx.x;
  const int b = (wg >> 3) & 7, h = (wg & 7) * 4 + (wg >> 6);
  const unsigned lds0 = (unsigned)(size_t)(LAS char*)lds;
  const int lw = wid & 3;
  const unsigned r8 = (lane >> 2) & 7, sub = lane >> 5;
  const unsigned xr = 16u * ((lane & 3) ^ ((2 * (lw & 1) + (r8 >> 2)) & 3));
  const unsigned vo_k = r8 * 256u + sub * 64u + xr, vo_v = r8 * 256u + sub * 64u + 16u * (lane & 3);
  const size_t kvoff = (size_t)(b * NH + h) * SK * DH * 2;
  const __amdgpu_buffer_rsrc_t rk = __builtin_amdgcn_make_buffer_rsrc((void*)(kc + kvoff), 0, SK * DH * 2, 0x00020000);
  const __amdgpu_buffer_rsrc_t rv = __builtin_amdgcn_make_buffer_rsrc((void*)(vc + kvoff), 0, SK * DH * 2, 0x00020000);
  auto issue_kv = [&](int t, int slot) {
    const unsigned sb = lds0 + slot * SLOT;
#pragma unroll
    for (int i = 0; i < 2; ++i) {
      const unsigned g = lw + 4 * i;
#pragma unroll
      for (int hf = 0; hf < 2; ++hf) dma16<KV_NT>(rk, vo_k, t * 16384u + g * 2048u + hf * 128u, sb + g * 2048u + hf * 1024u);
    }
#pragma unroll
    for (int i = 0; i < 2; ++i) {
      const unsigned g = lw + 4 * i;
#pragma unroll
      for (int hf = 0; hf < 2; ++hf) dma16<KV_NT>(rv, vo_v, t * 16384u + g * 2048u + hf * 128u, sb + 16384u + g * 2048u + hf * 1024u);
    }
  };
  if (wid >= 4) {
    const unsigned vo_x = r8 * 2048u + sub * 64u + xr, vo_w = r8 * 8192u + sub * 64u + 16u * (lane & 3);
    const __amdgpu_buffer_rsrc_t rx = __builtin_amdgcn_make_buffer_rsrc((void*)(x + (size_t)b * SQ * DM * 2), 0, SQ * DM * 2, 0x00020000);
    const __amdgpu_buffer_rsrc_t rw = __builtin_amdgcn_make_buffer_rsrc((void*)(wq + h * DH * 2), 0, DM * NH * DH * 2 - h * DH * 2, 0x00020000);
    auto issue_q = [&](int c, int slot) {
      const unsigned sb = lds0 + slot * SLOT;
#pragma unroll
      for (int i = 0; i < 4; ++i) { const unsigned g = lw + 4 * i; dma16<W_NT>(rx, vo_x, g * 16384u + c * 128u, sb + g * 1024u); }
#pragma unroll
      for (int i = 0; i < 2; ++i) {
        const unsigned g = lw + 4 * i;
#pragma unroll
        for (int hf = 0; hf < 2; ++hf) dma16<W_NT>(rw, vo_w, (c * 64u + g * 8u) * 8192u + hf * 128u, sb + 16384u + g * 2048u + hf * 1024u);
      }
    };
    issue_q(0, 0); issue_q(1, 1); issue_q(2, 2);
    WAITV_BAR(16);
    int sl = 0;
    for (int i = 1; i <= 13; ++i) { WAITV_BAR(8); issue_q(i + 2, sl); sl = sl == 2 ? 0 : sl + 1; }
    WAITV_BAR(8); issue_kv(2, kvslot(2));
    WAITV_BAR(8); issue_kv(3, kvslot(3));
#pragma unroll
    for (int t = 0; t <= 11; ++t) { WAITV_BAR(24); issue_kv(t + 4, kvslot(t + 4)); }
    WAITV_BAR(24);
    WAITV_BAR(16);
    WAITV_BAR(8);
    WAITV_BAR(0);
    return;
  }
  issue_kv(0, kvslot(0)); issue_kv(1, kvslot(1));
  const int r = lane & 31, hh = lane >> 5, qb = wid;
  const int tq = (lane >> 2) & 3, tp = lane & 3, blk = (lane >> 4) & 1;
  const LAS char* L = (const LAS char*)lds;
  const int rowe = 64 * (r & 7) + 16 * (hh ^ ((r >> 2) & 3));
  const int xbase_e = 1024 * (4 * qb + (r >> 3)) + rowe, xbase_o = xbase_e ^ 32;
  const int kbase_e = 2048 * (r >> 3) + rowe, kbase_o = kbase_e ^ 32;
  const int pp = (tp == 1) ? 2 : (tp == 2) ? 1 : tp;
  const int wbase = 16384 + hh * 2048 + tq * 64 + blk * 32 + pp * 8;
  const int vbase = 16384 + hh * 256 + tq * 64 + blk * 32 + tp * 8;
  f32x16 acc[4] = {};
  {
    int sl = 0;
    for (int c = 0; c < 16; ++c) {
      BAR_L();
      const LAS char* S = L + sl * SLOT;
#pragma unroll
      for (int ks = 0; ks < 4; ++ks) {
        const bf16x8 xf = rd128(S + ((ks & 1) ? xbase_o : xbase_e) + 512 * (ks >> 1));
#pragma unroll
        for (int db = 0; db < 4; ++db) {
          const bf16x8 wf = rdtr(S + wbase + 4096 * ks + 512 * db, S + wbase + 4096 * ks + 512 * db + 256);
          acc[db] = MFMA(wf, xf, acc[db]);
        }
      }
      sl = sl == 2 ? 0 : sl + 1;
    }
  }
  bf16x8 qf[8];
#pragma unroll
  for (int s = 0; s < 8; ++s) qf[s] = pack8(acc[s >> 1], s & 1);
  f32x16 o[4] = {};
  float m = -__builtin_inff(), l = 0.f;
  asm volatile("s_waitcnt vmcnt(0)" ::: "memory");
  int sidx = 0;
  for (int t = 0; t < 16; ++t) {
    BAR_L();
    const int slot = sidx == 0 ? 3 : sidx == 1 ? 4 : sidx == 2 ? 1 : sidx == 3 ? 2 : 0;
    sidx = sidx == 4 ? 0 : sidx + 1;
    const LAS char* S = L + slot * SLOT;
    f32x16 s0 = {}, s1 = {};
#pragma unroll
    for (int s = 0; s < 8; ++s) {
      const int off = ((s & 1) ? kbase_o : kbase_e) + 512 * (s >> 1);
      const bf16x8 k0 = rd128(S + off), k1 = rd128(S + off + 8192);
      s0 = MFMA(k0, qf[s], s0);
      s1 = MFMA(k1, qf[s], s1);
    }
    float tm = fmaxf(s0[0], s1[0]);
#pragma unroll
    for (int i = 1; i < 16; ++i) tm = fmaxf(tm, fmaxf(s0[i], s1[i]));
    tm *= C2;
    if (__any(tm - m > THR)) {
      const float tm2 = fmaxf(tm, swap_other(tm));
      const float mn = fmaxf(m, tm2);
      const float alpha = __builtin_amdgcn_exp2f(m - mn);
      m = mn; l *= alpha;
#pragma unroll
      for (int d = 0; d < 4; ++d)
#pragma unroll
        for (int i = 0; i < 16; ++i) o[d][i] *= alpha;
    }
    const float nm = -m;
    float ps = 0.f;
#pragma unroll
    for (int i = 0; i < 16; ++i) { s0[i] = __builtin_amdgcn_exp2f(fmaf(s0[i], C2, nm)); ps += s0[i]; }
#pragma unroll
    for (int i = 0; i < 16; ++i) { s1[i] = __builtin_amdgcn_exp2f(fmaf(s1[i], C2, nm)); ps += s1[i]; }
    l += ps;
    bf16x8 pf[4] = {pack8(s0, 0), pack8(s0, 1), pack8(s1, 0), pack8(s1, 1)};
#pragma unroll
    for (int ks = 0; ks < 4; ++ks)
#pragma unroll
      for (int db = 0; db < 4; ++db) {
        const bf16x8 vf = rdtr(S + vbase + 4096 * ks + 512 * db, S + vbase + 4096 * ks + 512 * db + 2048);
        o[db] = MFMA(vf, pf[ks], o[db]);
      }
  }
  l += swap_other(l);
  const float inv = 1.0f / l;
  char* op = ows + ((size_t)((b * 4 + qb) * 256 + h * 8)) * 1024 + lane * 16;
#pragma unroll
  for (int db = 0; db < 4; ++db) {
#pragma unroll
    for (int i = 0; i < 16; ++i) o[db][i] *= inv;
#pragma unroll
    for (int s = 0; s < 2; ++s) *(bf16x8*)(op + (db * 2 + s) * 1024) = pack8(o[db], s);
  }
}

__global__ __launch_bounds__(512, 2) void oproj_kernel(const char* __restrict__ ows, const char* __restrict__ wo, char* __restrict__ out) {
  __shared__ __attribute__((aligned(16))) char lds[LDS_BYTES];
  const int tid = threadIdx.x, lane = tid & 63;
  const int wid = rfl(tid >> 6);
  const int wg = blockIdx.x;
  const int xcd = wg & 7, j = wg >> 3;
  const int rb = (xcd >> 1) * 4 + (j >> 3), cb = (xcd & 1) * 8 + (j & 7);
  const unsigned lds0 = (unsigned)(size_t)(LAS char*)lds;
  const int lw = wid & 3;
  if (wid >= 4) {
    const unsigned r8 = (lane >> 2) & 7, sub = lane >> 5;
    const unsigned vo = r8 * 2048u + sub * 64u + 16u * (lane & 3);
    const __amdgpu_buffer_rsrc_t rw = __builtin_amdgcn_make_buffer_rsrc((void*)(wo + cb * 128), 0, NH * DH * DM * 2 - cb * 128, 0x00020000);
    auto issue = [&](int st, int slot) {
      const unsigned sb = lds0 + slot * SLOT + lw * 8192u;
#pragma unroll
      for (int i = 0; i < 8; ++i) dma16<W_NT>(rw, vo, (lw * 1024u + st * 64u + i * 8u) * 2048u, sb + i * 1024u);
    };
    issue(0, 0); issue(1, 1); issue(2, 2); issue(3, 3); issue(4, 4);
    WAITV_BAR(32);
    int sl = 0;
    for (int i = 1; i <= 11; ++i) { WAITV_BAR(24); issue(i + 4, sl); sl = sl == 4 ? 0 : sl + 1; }
    WAITV_BAR(24);
    WAITV_BAR(16);
    WAITV_BAR(8);
    WAITV_BAR(0);
    asm volatile("s_barrier" ::: "memory");
    asm volatile("s_barrier" ::: "memory");
    return;
  }
  const int hh = lane >> 5;
  const int tq = (lane >> 2) & 3, tp = lane & 3, blk = (lane >> 4) & 1;
  const LAS char* L = (const LAS char*)lds;
  const int abase = lw * 8192 + hh * 256 + tq * 64 + blk * 32 + tp * 8;
  const char* o0 = ows + ((size_t)(rb * 2 + 0) * 256 + lw * 64) * 1024 + lane * 16;
  const char* o1 = ows + ((size_t)(rb * 2 + 1) * 256 + lw * 64) * 1024 + lane * 16;
  f32x16 acc[2][2] = {};
  bf16x8 bA[4][2], bB[4][2];
#pragma unroll
  for (int ks = 0; ks < 4; ++ks) { bA[ks][0] = *(const bf16x8*)(o0 + ks * 1024); bA[ks][1] = *(const bf16x8*)(o1 + ks * 1024); }
  int sl = 0;
  for (int st = 0; st < 16; st += 2) {
#pragma unroll
    for (int ks = 0; ks < 4; ++ks) { bB[ks][0] = *(const bf16x8*)(o0 + ((st + 1) * 4 + ks) * 1024); bB[ks][1] = *(const bf16x8*)(o1 + ((st + 1) * 4 + ks) * 1024); }
    BAR_L();
    {
      const LAS char* S = L + sl * SLOT + abase;
#pragma unroll
      for (int ks = 0; ks < 4; ++ks)
#pragma unroll
        for (int nb = 0; nb < 2; ++nb) {
          const bf16x8 af = rdtr(S + 2048 * ks + 512 * nb, S + 2048 * ks + 512 * nb + 1024);
          acc[nb][0] = MFMA(af, bA[ks][0], acc[nb][0]);
          acc[nb][1] = MFMA(af, bA[ks][1], acc[nb][1]);
        }
      sl = sl == 4 ? 0 : sl + 1;
    }
    if (st + 2 < 16) {
#pragma unroll
      for (int ks = 0; ks < 4; ++ks) { bA[ks][0] = *(const bf16x8*)(o0 + ((st + 2) * 4 + ks) * 1024); bA[ks][1] = *(const bf16x8*)(o1 + ((st + 2) * 4 + ks) * 1024); }
    }
    BAR_L();
    {
      const LAS char* S = L + sl * SLOT + abase;
#pragma unroll
      for (int ks = 0; ks < 4; ++ks)
#pragma unroll
        for (int nb = 0; nb < 2; ++nb) {
          const bf16x8 af = rdtr(S + 2048 * ks + 512 * nb, S + 2048 * ks + 512 * nb + 1024);
          acc[nb][0] = MFMA(af, bB[ks][0], acc[nb][0]);
          acc[nb][1] = MFMA(af, bB[ks][1], acc[nb][1]);
        }
      sl = sl == 4 ? 0 : sl + 1;
    }
  }
  BAR_L();
  LAS char* W = (LAS char*)lds;
#pragma unroll
  for (int ti = 0; ti < 4; ++ti) {
    const f32x16& a = acc[ti & 1][ti >> 1];
#pragma unroll
    for (int g = 0; g < 4; ++g) { f32x4 v = {a[4 * g], a[4 * g + 1], a[4 * g + 2], a[4 * g + 3]}; *(LAS f32x4*)(W + (ti * 4 + lw) * 4096 + g * 1024 + lane * 16) = v; }
  }
  BAR_L();
  f32x4 sum[4];
#pragma unroll
  for (int g = 0; g < 4; ++g) {
    sum[g] = *(const LAS f32x4*)(L + (lw * 4 + 0) * 4096 + g * 1024 + lane * 16);
#pragma unroll
    for (int s = 1; s < 4; ++s) sum[g] += *(const LAS f32x4*)(L + (lw * 4 + s) * 4096 + g * 1024 + lane * 16);
  }
  const int nb = lw & 1, q2 = lw >> 1;
  const int row = rb * 64 + q2 * 32 + (lane & 31);
  char* orow = out + (size_t)row * DM * 2 + (cb * 64 + nb * 32) * 2;
#pragma unroll
  for (int g = 0; g < 4; g += 2) {
    unsigned a0 = cvtpk(sum[g][0], sum[g][1]), a1 = cvtpk(sum[g][2], sum[g][3]);
    unsigned b0 = cvtpk(sum[g + 1][0], sum[g + 1][1]), b1 = cvtpk(sum[g + 1][2], sum[g + 1][3]);
    auto r0 = __builtin_amdgcn_permlane32_swap(a0, b0, false, false);
    auto r1 = __builtin_amdgcn_permlane32_swap(a1, b1, false, false);
    u32x4 w = {r0[0], r1[0], r0[1], r1[1]};
    *(u32x4*)(orow + g * 16 + hh * 16) = w;
  }
}

extern "C" void kernel_launch(void* const* d_in, const int* in_sizes, int n_in, void* d_out, int out_size, void* d_ws, size_t ws_size, hipStream_t stream) {
  const char* x = (const char*)d_in[0]; const char* kc = (const char*)d_in[1]; const char* vc = (const char*)d_in[2];
  const char* wq = (const char*)d_in[3]; const char* wo = (const char*)d_in[4];
  attn_kernel<<<256, 512, 0, stream>>>(x, kc, vc, wq, (char*)d_ws);
  oproj_kernel<<<256, 512, 0, stream>>>((const char*)d_ws, wo, (char*)d_out);
}
```

```cpp
#include <hip/hip_runtime.h>
#include <hip/hip_bf16.h>

#define KV_NT 1
#define KV_AT 3
#define Q_ROT 0
#define O_AUX 16
#define O_ROT 0

typedef __bf16 bf16x8 __attribute__((ext_vector_type(8)));
typedef __bf16 bf16x2 __attribute__((ext_vector_type(2)));
typedef short s16x4 __attribute__((ext_vector_type(4)));
typedef short s16x8 __attribute__((ext_vector_type(8)));
typedef float f32x16 __attribute__((ext_vector_type(16)));
typedef float f32x4 __attribute__((ext_vector_type(4)));
typedef float f32x2 __attribute__((ext_vector_type(2)));
typedef unsigned u32x4 __attribute__((ext_vector_type(4)));
typedef unsigned long long u64;
#define LAS __attribute__((address_space(3)))
#define GAS __attribute__((address_space(1)))
#define DI __device__ __forceinline__
#define MFMA(a, b, c) __builtin_amdgcn_mfma_f32_32x32x16_bf16((a), (b), (c), 0, 0, 0)
#define SBAR() __builtin_amdgcn_sched_barrier(0)
#define PIN(x) asm volatile("" : "+v"(x))
#define RLX_AGENT __ATOMIC_RELAXED, __HIP_MEMORY_SCOPE_AGENT

constexpr int NB = 8, SQ = 128, DM = 1024, NH = 32, SK = 1024, DH = 128;
constexpr int SLOT = 32768, LDS_BYTES = 163840;
constexpr float C2 = 0.08838834764831845f * 1.4426950408889634f;
constexpr float THR = 8.0f;
constexpr unsigned WS_FLAGS = 8u << 20, WS_EPOCH = WS_FLAGS + 65536u;
constexpr u64 EPOCH_INC = 0x9E3779B97F4A7C15ull;
constexpr unsigned SPIN_CAP = 1u << 22;

#define STAMP(i) do {} while (0)

DI unsigned rfl(unsigned x) { return __builtin_amdgcn_readfirstlane(x); }
DI unsigned cvtpk(float lo, float hi) { f32x2 v = {lo, hi}; bf16x2 b = __builtin_convertvector(v, bf16x2); return __builtin_bit_cast(unsigned, b); }
DI bf16x8 pack8(const f32x16& x, int s) {
  u32x4 w = {cvtpk(x[8 * s + 0], x[8 * s + 1]), cvtpk(x[8 * s + 2], x[8 * s + 3]), cvtpk(x[8 * s + 4], x[8 * s + 5]), cvtpk(x[8 * s + 6], x[8 * s + 7])};
  return __builtin_bit_cast(bf16x8, w);
}
template <int NT> DI void dma16(__amdgpu_buffer_rsrc_t rs, unsigned voff, unsigned soff, unsigned ldsaddr) {
  if constexpr (NT) asm volatile("s_mov_b32 m0, %0\n\ts_nop 0\n\tbuffer_load_dwordx4 %1, %2, %3 offen nt lds" ::"s"(ldsaddr), "v"(voff), "s"(rs), "s"(soff) : "memory");
  else              asm volatile("s_mov_b32 m0, %0\n\ts_nop 0\n\tbuffer_load_dwordx4 %1, %2, %3 offen lds" ::"s"(ldsaddr), "v"(voff), "s"(rs), "s"(soff) : "memory");
}
DI bf16x8 rd128(const LAS char* p) { return *(const LAS bf16x8*)p; }
DI bf16x8 rdtr(const LAS char* lo, const LAS char* up) {
  s16x4 a = __builtin_amdgcn_ds_read_tr16_b64_v4i16((LAS s16x4*)lo);
  s16x4 b = __builtin_amdgcn_ds_read_tr16_b64_v4i16((LAS s16x4*)up);
  s16x8 c = __builtin_shufflevector(a, b, 0, 1, 2, 3, 4, 5, 6, 7);
  return __builtin_bit_cast(bf16x8, c);
}
#define BAR_L() asm volatile("s_waitcnt lgkmcnt(0)\n\ts_barrier" ::: "memory")
#define WAITV_BAR(N) asm volatile("s_waitcnt vmcnt(" #N ")\n\ts_barrier" ::: "memory")
#define MX3(a, b, c) __builtin_fmaxf(__builtin_fmaxf((a), (b)), (c))
DI float both_max(float x) { auto r = __builtin_amdgcn_permlane32_swap(__float_as_uint(x), __float_as_uint(x), false, false); return __builtin_fmaxf(__uint_as_float(r[0]), __uint_as_float(r[1])); }
DI float both_sum(float x) { auto r = __builtin_amdgcn_permlane32_swap(__float_as_uint(x), __float_as_uint(x), false, false); return __uint_as_float(r[0]) + __uint_as_float(r[1]); }
DI int nxt5(int s) { return s == 4 * SLOT ? 0 : s + SLOT; }

__global__ __launch_bounds__(512, 2) void mha_fused(const char* __restrict__ x, const char* __restrict__ kc, const char* __restrict__ vc,
                                                    const char* __restrict__ wq, const char* __restrict__ wo, char* __restrict__ out, char* __restrict__ ws) {
  __shared__ __attribute__((aligned(16))) char lds[LDS_BYTES];
  const int tid = threadIdx.x, lane = tid & 63;
  const int wid = rfl(tid >> 6);
  const int wg = blockIdx.x;
  const int xcd = wg & 7, j = wg >> 3;
  const int b = j & 7, h = xcd * 4 + (j >> 3);
  const int rb = (xcd >> 1) * 4 + (j >> 3), cb = (xcd & 1) * 8 + (j & 7);
  const int qrot = Q_ROT ? (2 * (j & 7) + 4 * (j >> 3)) & 15 : 0;
  const int orot = O_ROT ? (2 * (j & 7) + 4 * (j >> 3)) & 15 : 0;
  const unsigned lds0 = (unsigned)(size_t)(LAS char*)lds;
  const int lw = wid & 3;
  const unsigned r8 = (lane >> 2) & 7, sub = lane >> 5;
  const unsigned xr = 16u * ((lane & 3) ^ ((2 * (lw & 1) + (r8 >> 2)) & 3));
  const unsigned vo_k = r8 * 256u + sub * 64u + xr, vo_v = r8 * 256u + sub * 64u + 16u * (lane & 3);
  const size_t kvoff = (size_t)(b * NH + h) * SK * DH * 2;
  const __amdgpu_buffer_rsrc_t rk = __builtin_amdgcn_make_buffer_rsrc((void*)(kc + kvoff), 0, SK * DH * 2, 0x00020000);
  const __amdgpu_buffer_rsrc_t rv = __builtin_amdgcn_make_buffer_rsrc((void*)(vc + kvoff), 0, SK * DH * 2, 0x00020000);
  GAS u64* flags = (GAS u64*)(ws + WS_FLAGS);
  const u64 tag = __hip_atomic_load((GAS u64*)(ws + WS_EPOCH), RLX_AGENT) + EPOCH_INC;

  if (wid >= 4) {
    const unsigned vo_x = r8 * 2048u + sub * 64u + xr, vo_w = r8 * 8192u + sub * 64u + 16u * (lane & 3);
    const unsigned vo_o = r8 * 2048u + sub * 64u + 16u * (lane & 3);
    const __amdgpu_buffer_rsrc_t rx = __builtin_amdgcn_make_buffer_rsrc((void*)(x + (size_t)b * SQ * DM * 2), 0, SQ * DM * 2, 0x00020000);
    const __amdgpu_buffer_rsrc_t rw = __builtin_amdgcn_make_buffer_rsrc((void*)(wq + h * DH * 2), 0, DM * NH * DH * 2 - h * DH * 2, 0x00020000);
    const __amdgpu_buffer_rsrc_t ro = __builtin_amdgcn_make_buffer_rsrc((void*)(wo + cb * 128), 0, NH * DH * DM * 2 - cb * 128, 0x00020000);
    auto issue_q = [&](int c, int so) {
      const unsigned sb = lds0 + so; const unsigned cc = (c + qrot) & 15;
#pragma unroll
      for (int i = 0; i < 4; ++i) { const unsigned g = lw + 4 * i; dma16<0>(rx, vo_x, g * 16384u + cc * 128u, sb + g * 1024u); }
#pragma unroll
      for (int i = 0; i < 4; ++i) { const unsigned g = lw + 4 * (i >> 1), hf = i & 1; dma16<0>(rw, vo_w, (cc * 64u + g * 8u) * 8192u + hf * 128u, sb + 16384u + g * 2048u + hf * 1024u); }
    };
    auto issue_k = [&](int t, int so) {
#pragma unroll
      for (int i = 0; i < 4; ++i) { const unsigned g = lw + 4 * (i >> 1), hf = i & 1; dma16<KV_NT>(rk, vo_k, t * 16384u + g * 2048u + hf * 128u, lds0 + so + g * 2048u + hf * 1024u); }
    };
    auto issue_v = [&](int t, int so) {
#pragma unroll
      for (int i = 0; i < 4; ++i) { const unsigned g = lw + 4 * (i >> 1), hf = i & 1; dma16<KV_NT>(rv, vo_v, t * 16384u + g * 2048u + hf * 128u, lds0 + so + 16384u + g * 2048u + hf * 1024u); }
    };
    auto issue_w = [&](int st, int hs, int so) {
      const unsigned ss = (st + orot) & 15; const unsigned cw = 2 * hs + (lw >> 1);
#pragma unroll
      for (int i = 0; i < 4; ++i) { const unsigned pc = (lw & 1) * 4 + i; dma16<0>(ro, vo_o, (cw * 1024u + ss * 64u + pc * 8u) * 2048u, lds0 + so + cw * 8192u + pc * 1024u); }
    };
    issue_q(0, 2 * SLOT); issue_q(1, 0); issue_q(2, SLOT);
    WAITV_BAR(16);
    int sl = 2 * SLOT;
    for (int i = 1; i <= 13; ++i) { WAITV_BAR(8); issue_q(i + 2, sl); sl = sl == 2 * SLOT ? 0 : sl + SLOT; }
    WAITV_BAR(8); issue_k(2, 0);
    WAITV_BAR(4); issue_k(3, SLOT);
    WAITV_BAR(16); issue_k(4, 2 * SLOT); issue_v(2, 0);
    int sa = 3 * SLOT, sb_ = SLOT;
    for (int t = 0; t <= 10; ++t) { WAITV_BAR(16); issue_k(t + 5, sa); issue_v(t + 3, sb_); sa = nxt5(sa); sb_ = nxt5(sb_); }
    for (int t = 11; t <= 12; ++t) { WAITV_BAR(16); issue_w(t - 11, 0, sa); issue_v(t + 3, sb_); sa = nxt5(sa); sb_ = nxt5(sb_); }
    for (int t = 13; t <= 15; ++t) { WAITV_BAR(16); issue_w(t - 11, 0, sa); issue_w(t - 13, 1, sb_); sa = nxt5(sa); sb_ = nxt5(sb_); }
    WAITV_BAR(16);
    issue_w(3, 1, sb_); sb_ = nxt5(sb_); issue_w(4, 1, sb_);
    for (int s = 1; s <= 3; ++s) { WAITV_BAR(16); issue_w(s + 4, 0, sa); issue_w(s + 4, 1, sa); sa = nxt5(sa); }
    for (int s = 4; s <= 11; ++s) { WAITV_BAR(24); issue_w(s + 4, 0, sa); issue_w(s + 4, 1, sa); sa = nxt5(sa); }
    WAITV_BAR(24); WAITV_BAR(16); WAITV_BAR(8); WAITV_BAR(0);
    asm volatile("s_barrier" ::: "memory");
    asm volatile("s_barrier" ::: "memory");
    if (wg == 0 && wid == 4) {
#pragma unroll 1
      for (unsigned spins = 0; spins < SPIN_CAP; ++spins) {
        bool ok = true;
#pragma unroll
        for (int i = 0; i < 4; ++i) ok &= __hip_atomic_load(flags + (lane + 64 * i) * 16, RLX_AGENT) == tag;
        if (__all(ok)) break;
        __builtin_amdgcn_s_sleep(2);
      }
      if (lane == 0) __hip_atomic_store((GAS u64*)(ws + WS_EPOCH), tag, RLX_AGENT);
    }
    return;
  }
  STAMP(0);
  const int r = lane & 31, hh = lane >> 5, qb = wid;
  const int tq = (lane >> 2) & 3, tp = lane & 3, blk = (lane >> 4) & 1;
  const LAS char* L = (const LAS char*)lds;
  const int rowe = 64 * (r & 7) + 16 * (hh ^ ((r >> 2) & 3));
  bf16x8 qf[8];
  {
    const int xbase_e = 1024 * (4 * qb + (r >> 3)) + rowe, xbase_o = xbase_e ^ 32;
    const int pp = (tp == 1) ? 2 : (tp == 2) ? 1 : tp;
    const int wbase = 16384 + hh * 2048 + tq * 64 + blk * 32 + pp * 8;
    f32x16 acc[4] = {};
    int sl = 2 * SLOT;
#pragma unroll 1
    for (int c = 0; c < 16; ++c) {
      BAR_L();
      if (c == 0) STAMP(1);
      if (c == 4) STAMP(12);
      if (c == 8) STAMP(13);
      if (c == 12) STAMP(14);
      const LAS char* S = L + sl;
      bf16x8 xf[4], wf[4][4];
#define WRD(ks, db) wf[ks][db] = rdtr(S + wbase + 4096 * (ks) + 512 * (db), S + wbase + 4096 * (ks) + 512 * (db) + 256)
      xf[0] = rd128(S + xbase_e); WRD(0, 0); WRD(0, 1); WRD(0, 2); WRD(0, 3);
      xf[1] = rd128(S + xbase_o); xf[2] = rd128(S + xbase_e + 512); xf[3] = rd128(S + xbase_o + 512);
      SBAR();
      if (c >= KV_AT && c < KV_AT + 8) {
        const unsigned i = c - KV_AT;
        const unsigned t = i >> 2, g = lw + 4 * (i & 1);
        const unsigned sb = lds0 + (3 + t) * SLOT + g * 2048u, so = t * 16384u + g * 2048u;
        if ((i & 2) == 0) { dma16<KV_NT>(rk, vo_k, so, sb); dma16<KV_NT>(rk, vo_k, so + 128u, sb + 1024u); }
        else              { dma16<KV_NT>(rv, vo_v, so, sb + 16384u); dma16<KV_NT>(rv, vo_v, so + 128u, sb + 16384u + 1024u); }
      }
      SBAR();
#pragma unroll
      for (int ks = 0; ks < 4; ++ks) {
#pragma unroll
        for (int db = 0; db < 4; ++db) {
          acc[db] = MFMA(wf[ks][db], xf[ks], acc[db]);
          if (ks < 3) { WRD(ks + 1, db); }
          SBAR();
        }
      }
#undef WRD
      sl = sl == 2 * SLOT ? 0 : sl + SLOT;
    }
#pragma unroll
    for (int s = 0; s < 8; ++s) qf[s] = pack8(acc[s >> 1], s & 1);
  }
  STAMP(2);
  f32x16 o[4] = {};
  {
    const int kbase_e = 2048 * (r >> 3) + rowe, kbase_o = kbase_e ^ 32;
    const int vbase = 16384 + hh * 256 + tq * 64 + blk * 32 + tp * 8;
    float m, l = 0.f;
    f32x16 sA0, sA1, sB0, sB1;
    bf16x8 kf[16], vf[16];
    u32x4 pw[4];
    const f32x16 zero16 = {};
    int sk = 3 * SLOT;
    int sv = 3 * SLOT;
#define KRD(i, SP) kf[i] = rd128((SP) + ((((i) >> 1) & 1) ? kbase_o : kbase_e) + 512 * ((i) >> 2) + 8192 * ((i) & 1))
#define VRD(i, SP) vf[i] = rdtr((SP) + vbase + 4096 * ((i) >> 2) + 512 * ((i) & 3), (SP) + vbase + 4096 * ((i) >> 2) + 512 * ((i) & 3) + 2048)
#define EX(v) __builtin_amdgcn_exp2f(__builtin_fmaf((v), C2, nm))
#define PF(k) __builtin_bit_cast(bf16x8, pw[k])
    asm volatile("s_waitcnt vmcnt(0) lgkmcnt(0)\n\ts_barrier" ::: "memory");
    STAMP(3);
    {
      const LAS char* KP = L + sk;
#pragma unroll
      for (int i = 0; i < 16; ++i) KRD(i, KP);
      sA0 = MFMA(kf[0], qf[0], zero16); sA1 = MFMA(kf[1], qf[0], zero16);
#pragma unroll
      for (int s = 1; s < 8; ++s) { sA0 = MFMA(kf[2 * s], qf[s], sA0); sA1 = MFMA(kf[2 * s + 1], qf[s], sA1); }
      BAR_L();
      sk = nxt5(sk);
      float a = MX3(sA0[0], sA0[1], sA1[0]); a = MX3(a, sA1[1], sA0[2]);
#pragma unroll
      for (int i = 3; i < 16; i += 2) a = MX3(a, sA0[i], (i + 1 < 16) ? sA0[i + 1] : sA0[i]);
#pragma unroll
      for (int i = 2; i < 16; i += 2) a = MX3(a, sA1[i], sA1[i + 1]);
      m = both_max(a) * C2;
      const float nm = -m;
#pragma unroll
      for (int i = 0; i < 16; ++i) sA0[i] = EX(sA0[i]);
      const LAS char* KN = L + sk;
      KRD(0, KN); KRD(1, KN); KRD(2, KN); KRD(3, KN);
    }
#define GA(i, MF, BODY) do { MF; if ((i) + 4 < 16) { KRD((i) + 4, KP_); } BODY; SBAR(); } while (0)
#define A_EXP(Y1, e) do { Y1[e] = EX(Y1[e]); Y1[e + 1] = EX(Y1[e + 1]); PIN(Y1); } while (0)
#define A_SUM(Y, e) do { sacc += Y[e]; sacc += Y[e + 1]; sacc += Y[e + 2]; sacc += Y[e + 3]; PIN(sacc); } while (0)
#define A_PK(k, d, Y, e) do { pw[k][d] = cvtpk(Y[e], Y[e + 1]); pw[k][d + 1] = cvtpk(Y[e + 2], Y[e + 3]); PIN(pw[k]); } while (0)
#define PHASE_A(X0, X1, Y0, Y1) do { const LAS char* KP_ = L + sk; const LAS char* VP_ = L + sv; const float nm = -m; float sacc = l; SBAR(); \
      GA(0,  X0 = MFMA(kf[0],  qf[0], zero16), { A_EXP(Y1, 0);  A_PK(0, 0, Y0, 0); }); \
      GA(1,  X1 = MFMA(kf[1],  qf[0], zero16), { A_EXP(Y1, 2);  A_PK(0, 2, Y0, 4); }); \
      GA(2,  X0 = MFMA(kf[2],  qf[1], X0),     { A_EXP(Y1, 4);  A_PK(1, 0, Y0, 8); }); \
      GA(3,  X1 = MFMA(kf[3],  qf[1], X1),     { A_EXP(Y1, 6);  A_PK(1, 2, Y0, 12); }); \
      GA(4,  X0 = MFMA(kf[4],  qf[2], X0),     { A_EXP(Y1, 8);  }); \
      GA(5,  X1 = MFMA(kf[5],  qf[2], X1),     { A_EXP(Y1, 10); }); \
      GA(6,  X0 = MFMA(kf[6],  qf[3], X0),     { A_EXP(Y1, 12); }); \
      GA(7,  X1 = MFMA(kf[7],  qf[3], X1),     { A_EXP(Y1, 14); }); \
      GA(8,  X0 = MFMA(kf[8],  qf[4], X0),     { A_SUM(Y0, 0);  }); \
      GA(9,  X1 = MFMA(kf[9],  qf[4], X1),     { A_SUM(Y0, 4);  }); \
      GA(10, X0 = MFMA(kf[10], qf[5], X0),     { A_SUM(Y0, 8);  }); \
      GA(11, X1 = MFMA(kf[11], qf[5], X1),     { A_SUM(Y0, 12); }); \
      GA(12, X0 = MFMA(kf[12], qf[6], X0),     { VRD(0, VP_); A_SUM(Y1, 0);  A_PK(2, 0, Y1, 0); }); \
      GA(13, X1 = MFMA(kf[13], qf[6], X1),     { VRD(1, VP_); A_SUM(Y1, 4);  A_PK(2, 2, Y1, 4); }); \
      GA(14, X0 = MFMA(kf[14], qf[7], X0),     { VRD(2, VP_); A_SUM(Y1, 8);  A_PK(3, 0, Y1, 8); }); \
      GA(15, X1 = MFMA(kf[15], qf[7], X1),     { VRD(3, VP_); A_SUM(Y1, 12); A_PK(3, 2, Y1, 12); }); \
      l = sacc; } while (0)
#define GB(i, BODY) do { o[(i) & 3] = MFMA(vf[i], PF((i) >> 2), o[(i) & 3]); if ((i) + 4 < 16) { VRD((i) + 4, VP_); } BODY; SBAR(); } while (0)
#define B_EXP(X0, e) do { X0[e] = EX(X0[e]); X0[e + 1] = EX(X0[e + 1]); PIN(X0); } while (0)
#define PHASE_B(X0, X1, NEXTK) do { const LAS char* VP_ = L + sv; const LAS char* KN_ = L + sk; float a_, b_; float alpha_ = 1.f; bool resc_ = false; SBAR(); \
      GB(0, {}); \
      GB(1, {}); \
      GB(2, { a_ = MX3(X0[0], X0[1], X0[2]); a_ = MX3(a_, X0[3], X0[4]); a_ = MX3(a_, X0[5], X0[6]); a_ = MX3(a_, X0[7], X0[8]); PIN(a_); }); \
      GB(3, { a_ = MX3(a_, X0[9], X0[10]); a_ = MX3(a_, X0[11], X0[12]); a_ = MX3(a_, X0[13], X0[14]); b_ = MX3(X0[15], X1[0], X1[1]); PIN(a_); PIN(b_); }); \
      GB(4, { b_ = MX3(b_, X1[2], X1[3]); b_ = MX3(b_, X1[4], X1[5]); b_ = MX3(b_, X1[6], X1[7]); a_ = MX3(a_, X1[8], X1[9]); PIN(a_); PIN(b_); }); \
      GB(5, { b_ = MX3(b_, X1[10], X1[11]); a_ = MX3(a_, X1[12], X1[13]); b_ = MX3(b_, X1[14], X1[15]); a_ = __builtin_fmaf(__builtin_fmaxf(a_, b_), C2, -m); PIN(a_); }); \
      if (__builtin_expect(__any(a_ > THR), 0)) { const float dl_ = __builtin_fmaxf(both_max(a_), 0.f); m += dl_; alpha_ = __builtin_amdgcn_exp2f(-dl_); l *= alpha_; resc_ = true; } \
      const float nm = -m; SBAR(); \
      GB(6,  { B_EXP(X0, 0); }); \
      GB(7,  { B_EXP(X0, 2); }); \
      GB(8,  { B_EXP(X0, 4); }); \
      GB(9,  { B_EXP(X0, 6); }); \
      GB(10, { B_EXP(X0, 8); }); \
      GB(11, { B_EXP(X0, 10); }); \
      GB(12, { if (NEXTK) { KRD(0, KN_); } B_EXP(X0, 12); }); \
      GB(13, { if (NEXTK) { KRD(1, KN_); } B_EXP(X0, 14); }); \
      GB(14, { if (NEXTK) { KRD(2, KN_); } }); \
      GB(15, { if (NEXTK) { KRD(3, KN_); } }); \
      if (__builtin_expect(resc_, 0)) { _Pragma("unroll") for (int d_ = 0; d_ < 4; ++d_) _Pragma("unroll") for (int i_ = 0; i_ < 16; ++i_) o[d_][i_] *= alpha_; } \
      sv = nxt5(sv); } while (0)
#pragma unroll 1
    for (int t = 1; t < 15; t += 2) {
      PHASE_A(sB0, sB1, sA0, sA1); BAR_L(); sk = nxt5(sk); PHASE_B(sB0, sB1, true);
      PHASE_A(sA0, sA1, sB0, sB1); BAR_L(); sk = nxt5(sk); PHASE_B(sA0, sA1, true);
    }
    PHASE_A(sB0, sB1, sA0, sA1); BAR_L(); PHASE_B(sB0, sB1, false);
    {
      const LAS char* VP_ = L + sv; const float nm = -m;
#pragma unroll
      for (int i = 0; i < 16; ++i) VRD(i, VP_);
#pragma unroll
      for (int i = 0; i < 16; ++i) sB1[i] = EX(sB1[i]);
      float sacc = l;
#pragma unroll
      for (int i = 0; i < 16; ++i) sacc += sB0[i];
#pragma unroll
      for (int i = 0; i < 16; ++i) sacc += sB1[i];
      const bf16x8 p0 = pack8(sB0, 0), p1 = pack8(sB0, 1), p2 = pack8(sB1, 0), p3 = pack8(sB1, 1);
#pragma unroll
      for (int db = 0; db < 4; ++db) o[db] = MFMA(vf[db], p0, o[db]);
#pragma unroll
      for (int db = 0; db < 4; ++db) o[db] = MFMA(vf[4 + db], p1, o[db]);
#pragma unroll
      for (int db = 0; db < 4; ++db) o[db] = MFMA(vf[8 + db], p2, o[db]);
#pragma unroll
      for (int db = 0; db < 4; ++db) o[db] = MFMA(vf[12 + db], p3, o[db]);
      l = both_sum(sacc);
    }
    STAMP(7);
    const float inv = 1.0f / l;
    const unsigned obase = (unsigned)(((b * 4 + qb) * 256 + h * 8) * 1024);
    const __amdgpu_buffer_rsrc_t rs = __builtin_amdgcn_make_buffer_rsrc((void*)(ws + obase), 0, 8192, 0x00020000);
#pragma unroll
    for (int db = 0; db < 4; ++db) {
#pragma unroll
      for (int i = 0; i < 16; ++i) o[db][i] *= inv;
#pragma unroll
      for (int s = 0; s < 2; ++s) __builtin_amdgcn_raw_buffer_store_b128(__builtin_bit_cast(u32x4, pack8(o[db], s)), rs, lane * 16 + (db * 2 + s) * 1024, 0, 16);
    }
    asm volatile("s_waitcnt vmcnt(0) lgkmcnt(0)\n\ts_barrier" ::: "memory");
    if (tid == 0) __hip_atomic_store(flags + (b * NH + h) * 16, tag, RLX_AGENT);
  }
  STAMP(8);
  {
    const int ob = rb >> 1;
#pragma unroll 1
    for (unsigned spins = 0; spins < SPIN_CAP; ++spins) {
      const u64 v = __hip_atomic_load(flags + (ob * NH + 8 * lw + (lane & 7)) * 16, RLX_AGENT);
      if (__all(v == tag)) break;
      __builtin_amdgcn_s_sleep(1);
    }
    __builtin_amdgcn_fence(__ATOMIC_ACQUIRE, "wavefront");
    STAMP(9);
    const __amdgpu_buffer_rsrc_t rs = __builtin_amdgcn_make_buffer_rsrc((void*)ws, 0, 8 << 20, 0x00020000);
    const unsigned ob0 = (unsigned)((rb * 2 * 256 + lw * 64) * 1024);
    const int abase = lw * 8192 + hh * 256 + tq * 64 + blk * 32 + tp * 8;
    f32x16 acc[2][2] = {};
    u32x4 bq[4][8];
#define OLD(buf, st) do { const unsigned so_ = ob0 + (unsigned)((((st) + orot) & 15) * 4096); _Pragma("unroll") for (int i_ = 0; i_ < 8; ++i_) \
      bq[buf][i_] = __builtin_amdgcn_raw_buffer_load_b128(rs, lane * 16 + (i_ >> 1) * 1024, so_ + (i_ & 1) * 262144u, O_AUX); } while (0)
#define ARD(i_) af[i_] = rdtr(S_ + 2048 * ((i_) >> 1) + 512 * ((i_) & 1), S_ + 2048 * ((i_) >> 1) + 512 * ((i_) & 1) + 1024)
#define OST(buf) do { const LAS char* S_ = L + sw + abase; bf16x8 af[8]; \
      ARD(0); ARD(1); ARD(2); ARD(3); SBAR(); \
      _Pragma("unroll") for (int i_ = 0; i_ < 8; ++i_) { \
        acc[i_ & 1][0] = MFMA(af[i_], __builtin_bit_cast(bf16x8, bq[buf][(i_ >> 1) * 2]), acc[i_ & 1][0]); \
        if (i_ < 4) { ARD(i_ + 4); } \
        acc[i_ & 1][1] = MFMA(af[i_], __builtin_bit_cast(bf16x8, bq[buf][(i_ >> 1) * 2 + 1]), acc[i_ & 1][1]); SBAR(); } \
      sw = nxt5(sw); } while (0)
    int sw = 4 * SLOT;
    OLD(0, 0); OLD(1, 1); OLD(2, 2); OLD(3, 3);
#pragma unroll 1
    for (int st = 0; st < 12; st += 4) {
      OST(0); OLD(0, st + 4); BAR_L();
      OST(1); OLD(1, st + 5); BAR_L();
      OST(2); OLD(2, st + 6); BAR_L();
      OST(3); OLD(3, st + 7); BAR_L();
    }
    OST(0); BAR_L(); OST(1); BAR_L(); OST(2); BAR_L(); OST(3);
    STAMP(10);
    BAR_L();
    LAS char* W = (LAS char*)lds;
#pragma unroll
    for (int ti = 0; ti < 4; ++ti) {
      const f32x16& a = acc[ti & 1][ti >> 1];
#pragma unroll
      for (int g = 0; g < 4; ++g) { f32x4 v = {a[4 * g], a[4 * g + 1], a[4 * g + 2], a[4 * g + 3]}; *(LAS f32x4*)(W + (ti * 4 + lw) * 4096 + g * 1024 + lane * 16) = v; }
    }
    BAR_L();
    f32x4 sum[4];
#pragma unroll
    for (int g = 0; g < 4; ++g) {
      sum[g] = *(const LAS f32x4*)(L + (lw * 4 + 0) * 4096 + g * 1024 + lane * 16);
#pragma unroll
      for (int s = 1; s < 4; ++s) sum[g] += *(const LAS f32x4*)(L + (lw * 4 + s) * 4096 + g * 1024 + lane * 16);
    }
    const int nb = lw & 1, q2 = lw >> 1;
    const int row = rb * 64 + q2 * 32 + r;
    char* orow = out + (size_t)row * DM * 2 + (cb * 64 + nb * 32) * 2;
#pragma unroll
    for (int g = 0; g < 4; g += 2) {
      unsigned a0 = cvtpk(sum[g][0], sum[g][1]), a1 = cvtpk(sum[g][2], sum[g][3]);
      unsigned b0 = cvtpk(sum[g + 1][0], sum[g + 1][1]), b1 = cvtpk(sum[g + 1][2], sum[g + 1][3]);
      auto r0 = __builtin_amdgcn_permlane32_swap(a0, b0, false, false);
      auto r1 = __builtin_amdgcn_permlane32_swap(a1, b1, false, false);
      u32x4 w = {r0[0], r1[0], r0[1], r1[1]};
      *(u32x4*)(orow + g * 16 + hh * 16) = w;
    }
    STAMP(11);
  }
}

extern "C" void kernel_launch(void* const* d_in, const int* in_sizes, int n_in, void* d_out, int out_size, void* d_ws, size_t ws_size, hipStream_t stream) {
  mha_fused<<<256, 512, 0, stream>>>((const char*)d_in[0], (const char*)d_in[1], (const char*)d_in[2], (const char*)d_in[3], (const char*)d_in[4], (char*)d_out, (char*)d_ws);
}
```

```cpp
#include <hip/hip_runtime.h>
#include <hip/hip_bf16.h>

#define KV_AUX 2
#define KV_A 12
#define KV_ROT 1
#define KV_B 2
#define NB_R 3
#define W_THIN 1
#define W_STG 0
#define W_ROT 0
#define GROUPS 1
#define Q_ROT 1
#define O_ROT 0
#define OLD_AUX 16
#define OST_AUX 16
#define W_AT 5
#define CODE_KB 24

typedef __bf16 bf16x8 __attribute__((ext_vector_type(8)));
typedef __bf16 bf16x2 __attribute__((ext_vector_type(2)));
typedef short s16x4 __attribute__((ext_vector_type(4)));
typedef short s16x8 __attribute__((ext_vector_type(8)));
typedef float f32x16 __attribute__((ext_vector_type(16)));
typedef float f32x4 __attribute__((ext_vector_type(4)));
typedef float f32x2 __attribute__((ext_vector_type(2)));
typedef unsigned u32x4 __attribute__((ext_vector_type(4)));
typedef unsigned long long u64;
#define LAS __attribute__((address_space(3)))
#define GAS __attribute__((address_space(1)))
#define DI __device__ __forceinline__
#define MFMA(a, b, c) __builtin_amdgcn_mfma_f32_32x32x16_bf16((a), (b), (c), 0, 0, 0)
#define SBAR() __builtin_amdgcn_sched_barrier(0)
#define PIN(x) asm volatile("" : "+v"(x))
#define RLX_AGENT __ATOMIC_RELAXED, __HIP_MEMORY_SCOPE_AGENT

constexpr int NB = 8, SQ = 128, DM = 1024, NH = 32, SK = 1024, DH = 128;
constexpr int SLOT = 32768, LDS_BYTES = 163840;
constexpr float C2 = 0.08838834764831845f * 1.4426950408889634f;
constexpr float THR = 8.0f;
constexpr unsigned WS_FLAGS = 8u << 20, WS_EPOCH = WS_FLAGS + 65536u;
constexpr u64 EPOCH_INC = 0x9E3779B97F4A7C15ull;
constexpr unsigned SPIN_CAP = 1u << 22;

#define STAMP(i) do {} while (0)
#define LS() do {} while (0)

DI unsigned rfl(unsigned x) { return __builtin_amdgcn_readfirstlane(x); }
DI unsigned cvtpk(float lo, float hi) { f32x2 v = {lo, hi}; bf16x2 b = __builtin_convertvector(v, bf16x2); return __builtin_bit_cast(unsigned, b); }
DI bf16x8 pack8(const f32x16& x, int s) {
  u32x4 w = {cvtpk(x[8 * s + 0], x[8 * s + 1]), cvtpk(x[8 * s + 2], x[8 * s + 3]), cvtpk(x[8 * s + 4], x[8 * s + 5]), cvtpk(x[8 * s + 6], x[8 * s + 7])};
  return __builtin_bit_cast(bf16x8, w);
}
template <int AUX> DI void dma16(__amdgpu_buffer_rsrc_t rs, unsigned voff, unsigned soff, unsigned ldsaddr) {
  if constexpr (AUX == 2) asm volatile("s_mov_b32 m0, %0\n\ts_nop 0\n\tbuffer_load_dwordx4 %1, %2, %3 offen nt lds" ::"s"(ldsaddr), "v"(voff), "s"(rs), "s"(soff) : "memory");
  else                    asm volatile("s_mov_b32 m0, %0\n\ts_nop 0\n\tbuffer_load_dwordx4 %1, %2, %3 offen lds" ::"s"(ldsaddr), "v"(voff), "s"(rs), "s"(soff) : "memory");
}
#define DMA_PC(V, NTS) "buffer_load_dwordx4 " V ", %4, %0 offen " NTS "lds\n\t"
#define DMA_2(VA, VB, NTS) DMA_PC(VA, NTS) "s_add_u32 m0, m0, 0x400\n\ts_add_u32 %0, %0, %5\n\t" DMA_PC(VB, NTS) "s_add_u32 m0, m0, 0x400\n\ts_add_u32 %0, %0, %6\n\t"
template <int AUX, int SEL> DI void dma_tile(__amdgpu_buffer_rsrc_t rs, unsigned voe, unsigned voo, unsigned soff, unsigned p1, unsigned p2, unsigned ldsaddr) {
  const unsigned d2 = p2 - p1;
#define DMA_BODY(NTS) do { if constexpr (SEL == 0) asm volatile("s_mov_b32 m0, %1\n\ts_nop 0\n\t" DMA_2("%2", "%3", NTS) DMA_2("%2", "%3", NTS) DMA_2("%2", "%3", NTS) DMA_2("%2", "%3", NTS) DMA_2("%2", "%3", NTS) DMA_2("%2", "%3", NTS) DMA_2("%2", "%3", NTS) DMA_2("%2", "%3", NTS) \
        : "+s"(soff) : "s"(ldsaddr), "v"(voe), "v"(voo), "s"(rs), "s"(p1), "s"(d2) : "memory", "scc"); \
    else asm volatile("s_mov_b32 m0, %1\n\ts_nop 0\n\t" DMA_2("%2", "%2", NTS) DMA_2("%3", "%3", NTS) DMA_2("%2", "%2", NTS) DMA_2("%3", "%3", NTS) DMA_2("%2", "%2", NTS) DMA_2("%3", "%3", NTS) DMA_2("%2", "%2", NTS) DMA_2("%3", "%3", NTS) \
        : "+s"(soff) : "s"(ldsaddr), "v"(voe), "v"(voo), "s"(rs), "s"(p1), "s"(d2) : "memory", "scc"); } while (0)
  if constexpr (AUX == 2) DMA_BODY("nt "); else DMA_BODY("");
}
DI bf16x8 rd128(const LAS char* p) { return *(const LAS bf16x8*)p; }
DI bf16x8 rdtr(const LAS char* lo, const LAS char* up) {
  s16x4 a = __builtin_amdgcn_ds_read_tr16_b64_v4i16((LAS s16x4*)lo);
  s16x4 b = __builtin_amdgcn_ds_read_tr16_b64_v4i16((LAS s16x4*)up);
  s16x8 c = __builtin_shufflevector(a, b, 0, 1, 2, 3, 4, 5, 6, 7);
  return __builtin_bit_cast(bf16x8, c);
}
#define BAR() asm volatile("s_barrier" ::: "memory")
#define BAR_L() asm volatile("s_waitcnt lgkmcnt(0)\n\ts_barrier" ::: "memory")
#define WAITV_BAR(N) asm volatile("s_waitcnt vmcnt(" #N ")\n\ts_barrier" ::: "memory")
#define MX3(a, b, c) __builtin_fmaxf(__builtin_fmaxf((a), (b)), (c))
DI float both_max(float x) { auto r = __builtin_amdgcn_permlane32_swap(__float_as_uint(x), __float_as_uint(x), false, false); return __builtin_fmaxf(__uint_as_float(r[0]), __uint_as_float(r[1])); }
DI float both_sum(float x) { auto r = __builtin_amdgcn_permlane32_swap(__float_as_uint(x), __float_as_uint(x), false, false); return __uint_as_float(r[0]) + __uint_as_float(r[1]); }
DI void mfma_acc(f32x16& d, const bf16x8& a, const bf16x8& b) { asm volatile("v_mfma_f32_32x32x16_bf16 %0, %1, %2, %0" : "+v"(d) : "v"(a), "v"(b) : "memory"); }
DI void mfma_new(f32x16& d, const bf16x8& a, const bf16x8& b, const f32x16& c) { asm volatile("v_mfma_f32_32x32x16_bf16 %0, %1, %2, %3" : "=&v"(d) : "v"(a), "v"(b), "v"(c) : "memory"); }
DI float aexp(float x) { asm volatile("v_exp_f32 %0, %0" : "+v"(x) :: "memory"); return x; }
DI unsigned acvt(float a, float b) { unsigned r; asm volatile("v_cvt_pk_bf16_f32 %0, %1, %2" : "=v"(r) : "v"(a), "v"(b) : "memory"); return r; }
DI void aadd1(float& l, float a) { asm volatile("v_add_f32 %0, %0, %1" : "+v"(l) : "v"(a) : "memory"); }
DI void aadd2(float& l, float a, float b) { asm volatile("v_add_f32 %0, %0, %1\n\tv_add_f32 %0, %0, %2" : "+v"(l) : "v"(a), "v"(b) : "memory"); }
DI void aadd3(float& l, float a, float b, float c) { asm volatile("v_add_f32 %0, %0, %1\n\tv_add_f32 %0, %0, %2\n\tv_add_f32 %0, %0, %3" : "+v"(l) : "v"(a), "v"(b), "v"(c) : "memory"); }
DI void aadd4(float& l, float a, float b, float c, float d) { asm volatile("v_add_f32 %0, %0, %1\n\tv_add_f32 %0, %0, %2\n\tv_add_f32 %0, %0, %3\n\tv_add_f32 %0, %0, %4" : "+v"(l) : "v"(a), "v"(b), "v"(c), "v"(d) : "memory"); }
DI float amax7(float a, float b, float c, float d, float e, float f, float g) { float r; asm volatile("v_max3_f32 %0, %1, %2, %3\n\tv_max3_f32 %0, %0, %4, %5\n\tv_max3_f32 %0, %0, %6, %7" : "=&v"(r) : "v"(a), "v"(b), "v"(c), "v"(d), "v"(e), "v"(f), "v"(g) : "memory"); return r; }
DI void amax6(float& r, float a, float b, float c, float d, float e, float f) { asm volatile("v_max3_f32 %0, %0, %1, %2\n\tv_max3_f32 %0, %0, %3, %4\n\tv_max3_f32 %0, %0, %5, %6" : "+v"(r) : "v"(a), "v"(b), "v"(c), "v"(d), "v"(e), "v"(f) : "memory"); }
DI void amax8(float& r, float a, float b, float c, float d, float e, float f, float g, float h) { asm volatile("v_max3_f32 %0, %0, %1, %2\n\tv_max3_f32 %0, %0, %3, %4\n\tv_max3_f32 %0, %0, %5, %6\n\tv_max3_f32 %0, %0, %7, %8" : "+v"(r) : "v"(a), "v"(b), "v"(c), "v"(d), "v"(e), "v"(f), "v"(g), "v"(h) : "memory"); }
DI void amax3z(float& r, float a, float b, float c) { asm volatile("v_max3_f32 %0, %0, %1, %2\n\tv_max3_f32 %0, %0, %3, 0" : "+v"(r) : "v"(a), "v"(b), "v"(c) : "memory"); }
DI int nxt5(int s) { return s == 4 * SLOT ? 0 : s + SLOT; }
struct Tile { u32x4 v[16]; };
DI void tile_init(Tile& t) {
#pragma unroll
  for (int p = 0; p < 16; ++p) asm volatile("" : "=v"(t.v[p]));
}
template <int AUX, int IMM> DI void ld16(u32x4& d, __amdgpu_buffer_rsrc_t rs, unsigned voff, unsigned soff) {
  if constexpr (AUX == 2) asm volatile("buffer_load_dwordx4 %0, %1, %2, %3 offen offset:%4 nt" : "+v"(d) : "v"(voff), "s"(rs), "s"(soff), "i"(IMM) : "memory");
  else                    asm volatile("buffer_load_dwordx4 %0, %1, %2, %3 offen offset:%4" : "+v"(d) : "v"(voff), "s"(rs), "s"(soff), "i"(IMM) : "memory");
}
template <int P> DI void wr16(const u32x4& d, unsigned addr) { asm volatile("ds_write_b128 %0, %1 offset:%2" ::"v"(addr), "v"(d), "i"(P * 1024) : "memory"); }
template <int N> DI void tile_wr(const Tile& t, unsigned addr) {
  if constexpr (N == 32) asm volatile("s_waitcnt vmcnt(32)" ::: "memory"); else if constexpr (N == 16) asm volatile("s_waitcnt vmcnt(16)" ::: "memory"); else asm volatile("s_waitcnt vmcnt(0)" ::: "memory");
  wr16<0>(t.v[0], addr); wr16<1>(t.v[1], addr); wr16<2>(t.v[2], addr); wr16<3>(t.v[3], addr); wr16<4>(t.v[4], addr); wr16<5>(t.v[5], addr); wr16<6>(t.v[6], addr); wr16<7>(t.v[7], addr);
  wr16<8>(t.v[8], addr); wr16<9>(t.v[9], addr); wr16<10>(t.v[10], addr); wr16<11>(t.v[11], addr); wr16<12>(t.v[12], addr); wr16<13>(t.v[13], addr); wr16<14>(t.v[14], addr); wr16<15>(t.v[15], addr);
}

__global__ __launch_bounds__(512, 2) void mha_fused(const char* __restrict__ x, const char* __restrict__ kc, const char* __restrict__ vc,
                                                    const char* __restrict__ wq, const char* __restrict__ wo, char* out, char* __restrict__ ws) {
  __shared__ __attribute__((aligned(16))) char lds[LDS_BYTES];
  u64 pc0; asm volatile("s_getpc_b64 %0" : "=s"(pc0));
  const int tid = threadIdx.x, lane = tid & 63;
  const int wid = rfl(tid >> 6);
  const int wg = blockIdx.x;
  const int xcd = wg & 7, j = wg >> 3;
  const int b = j & 7, h = xcd * 4 + (j >> 3);
  const int rb = (xcd >> 1) * 4 + (j >> 3), cb = (xcd & 1) * 8 + (j & 7);
  const int ob = rb >> 1;
  const int wrot = W_ROT ? (rb & 3) : 0;
  auto smap = [&](int st) { const int p = st >> 1; return 2 * (2 * ((p + wrot) & 3) + ((ob & 1) ^ (p >> 2))) + (st & 1); };
  const unsigned lds0 = (unsigned)(size_t)(LAS char*)lds;
  GAS u64* flags = (GAS u64*)(ws + WS_FLAGS);

  if (wid >= 4) {
    const unsigned r8 = (lane >> 2) & 7, sub = lane >> 5;
    const unsigned hb = wid & 1;
    const unsigned c4 = lane & 3;
    const unsigned xe = hb ? c4 : (c4 ^ (r8 >> 2)), xo = hb ? c4 : (c4 ^ (2 + (r8 >> 2)));
    const unsigned wdst = lds0 + hb * 16384u + lane * 16u;
    const __amdgpu_buffer_rsrc_t rwo = __builtin_amdgcn_make_buffer_rsrc((void*)(wo + cb * 128), 0, NH * DH * DM * 2 - cb * 128, 0x00020000);
    const unsigned vow = r8 * 2048u + sub * 64u + 16u * c4 + hb * (2048u * 2048u);
#define WROW 2048u
    auto w_ld = [&](Tile& R, int st) {
      const unsigned s0 = (unsigned)smap(st) * (64u * WROW);
#pragma unroll
      for (int p = 0; p < 16; ++p) ld16<0, 0>(R.v[p], rwo, vow, s0 + (p >> 3) * (1024u * WROW) + (p & 7) * (8u * WROW));
    };
    if (wid >= 6) {
      const bool grpB = GROUPS == 1 ? ((j ^ (j >> 3)) & 1) : (GROUPS == 2);
      const size_t kvoff = (size_t)(b * NH + h) * SK * DH * 2;
      const __amdgpu_buffer_rsrc_t rs = __builtin_amdgcn_make_buffer_rsrc((void*)((hb ? vc : kc) + kvoff), 0, SK * DH * 2, 0x00020000);
      const unsigned voe = r8 * 256u + sub * 64u + 16u * xe, voo = r8 * 256u + sub * 64u + 16u * xo;
      const unsigned ldh = lds0 + hb * 16384u;
      const int krot = KV_ROT == 1 ? (j + 5 * xcd) & 15 : KV_ROT == 2 ? (wg * 7 + (wg >> 4)) & 15 : 0;
      auto tile_dma = [&](int t, int so) { dma_tile<KV_AUX, 1>(rs, voe, voo, ((t + krot) & 15) * 16384u, 128u, 2048u, ldh + so); };
      auto w_dma = [&](int st, int so) {
        const unsigned s0 = (unsigned)smap(st) * (64u * WROW);
#pragma unroll
        for (int p = 0; p < 16; ++p) dma16<0>(rwo, vow, s0 + (p >> 3) * (1024u * WROW) + (p & 7) * (8u * WROW), ldh + so + p * 1024u);
      };
      if (!grpB) {
        const GAS char* pc = (const GAS char*)(pc0 & ~255ull);
        const int part = ((j >> 1) & 3) * 2 + hb;
        u32x4 t0 = *(const GAS u32x4*)(pc + (part * (CODE_KB / 8)) * 1024 + lane * 16);
#pragma unroll
        for (int i = 1; i < CODE_KB / 8; ++i) t0 ^= *(const GAS u32x4*)(pc + (part * (CODE_KB / 8) + i) * 1024 + lane * 16);
        asm volatile("" ::"v"(t0));
      }
      if (grpB) {
        Tile R0, R1, R2;
        tile_init(R0); tile_init(R1); tile_init(R2);
        auto tile_ld = [&](Tile& R, int t) {
#pragma unroll
          for (int q = 0; q < 4; ++q) {
            const unsigned so = ((t + krot) & 15) * 16384u + q * 4096u;
            ld16<KV_AUX, 0>(R.v[4 * q], rs, voe, so); ld16<KV_AUX, 128>(R.v[4 * q + 1], rs, voe, so);
            ld16<KV_AUX, 2048>(R.v[4 * q + 2], rs, voo, so); ld16<KV_AUX, 2048 + 128>(R.v[4 * q + 3], rs, voo, so);
          }
        };
#pragma unroll 1
        for (int c = -1; c <= KV_B; ++c) BAR();
        tile_dma(0, 3 * SLOT); tile_dma(1, 4 * SLOT);
        if (NB_R > 0) tile_ld(R0, 2);
        if (NB_R > 1) { asm volatile("s_waitcnt vmcnt(32)" ::: "memory"); tile_ld(R1, 3); }
        if (NB_R > 2) { asm volatile("s_waitcnt vmcnt(32)" ::: "memory"); tile_ld(R2, 4); }
#pragma unroll 1
        for (int c = KV_B + 1; c <= 13; ++c) BAR();
        asm volatile("s_waitcnt vmcnt(0)" ::: "memory");
        if (NB_R > 0) tile_wr<0>(R0, wdst);
        BAR_L(); if (NB_R > 1) tile_wr<0>(R1, wdst + SLOT);     if (NB_R < 1 && !hb) tile_dma(2, 0);
        BAR_L(); if (NB_R > 2) tile_wr<0>(R2, wdst + 2 * SLOT); if (NB_R < 2 && !hb) tile_dma(3, SLOT); if (NB_R < 1 && hb) tile_dma(2, 0);
        BAR_L(); if (NB_R < 3 && !hb) tile_dma(4, 2 * SLOT); if (NB_R < 2 && hb) tile_dma(3, SLOT);
        if (hb) { BAR(); if (NB_R < 3) tile_dma(4, 2 * SLOT); }
      } else {
#pragma unroll 1
        for (int c = -1; c <= KV_A; ++c) BAR();
        __builtin_amdgcn_s_sleep(8);
        tile_dma(0, 3 * SLOT); tile_dma(1, 4 * SLOT);
#pragma unroll 1
        for (int c = KV_A + 1; c <= 13 + (int)hb; ++c) BAR();
        int sl = 0;
#pragma unroll 1
        for (int n = 2; n <= 4; ++n) { WAITV_BAR(32); tile_dma(n, sl); sl += SLOT; }
      }
      int sl = 3 * SLOT;
#pragma unroll 1
      for (int n = 5; n <= 15; ++n) { WAITV_BAR(32); tile_dma(n, sl); sl = nxt5(sl); }
      if (!hb) { WAITV_BAR(32); w_dma(0, sl); sl = nxt5(sl); }
#pragma unroll 1
      for (int s = 1 - (int)hb; s <= 3 - (int)hb; ++s) { WAITV_BAR(32); w_dma(s, sl); sl = nxt5(sl); }
      if (!hb) { asm volatile("s_waitcnt vmcnt(47)" ::: "memory"); w_dma(4, sl); }
      Tile R0, R1, R2;
      tile_init(R0); tile_init(R1); tile_init(R2);
      WAITV_BAR(16);
      if (hb) { w_dma(3, sl); sl = nxt5(sl); w_dma(4, sl); }
      WAITV_BAR(32); w_ld(R0, 8);
      WAITV_BAR(16); w_ld(R1, 9);
      BAR(); w_ld(R2, 10);
      BAR();   tile_wr<32>(R0, wdst + 2 * SLOT);
      BAR_L(); tile_wr<16>(R1, wdst + 3 * SLOT);
      BAR_L(); tile_wr<0>(R2, wdst + 4 * SLOT);
      sl = 0;
#pragma unroll 1
      for (int s = 11; s <= 15; ++s) { asm volatile("s_waitcnt vmcnt(47) lgkmcnt(0)\n\ts_barrier" ::: "memory"); w_dma(s, sl); sl += SLOT; }
      WAITV_BAR(47); WAITV_BAR(32); WAITV_BAR(16); WAITV_BAR(0); BAR();
      BAR();
      return;
    }
    const u64 tag = __hip_atomic_load((GAS u64*)(ws + WS_EPOCH), RLX_AGENT) + EPOCH_INC;
    {
      const int qrot = Q_ROT ? (2 * (j & 7) + 4 * (j >> 3)) & 15 : 0;
      const __amdgpu_buffer_rsrc_t rs = hb ? __builtin_amdgcn_make_buffer_rsrc((void*)(wq + h * DH * 2), 0, DM * NH * DH * 2 - h * DH * 2, 0x00020000)
                                           : __builtin_amdgcn_make_buffer_rsrc((void*)(x + (size_t)b * SQ * DM * 2), 0, SQ * DM * 2, 0x00020000);
      const unsigned rstr = hb ? 8192u : 2048u;
      const unsigned voe = r8 * rstr + sub * 64u + 16u * xe, voo = r8 * rstr + sub * 64u + 16u * xo;
      const unsigned cstr = hb ? 64u * 8192u : 128u;
      const unsigned p1 = hb ? 128u : 16384u, p2 = hb ? 65536u : 32768u;
      auto issue_q = [&](int c, int so) { dma_tile<0, 0>(rs, voe, voo, ((c + qrot) & 15) * cstr, p1, p2, lds0 + so + hb * 16384u); };
      issue_q(0, 2 * SLOT); issue_q(1, 0); issue_q(2, SLOT);
      { WAITV_BAR(32); LS(); }
      int sl = 2 * SLOT;
#pragma unroll 1
      for (int c = 0; c <= 12; ++c) { { WAITV_BAR(16); LS(); } issue_q(c + 3, sl); sl = sl == 2 * SLOT ? 0 : sl + SLOT; }
      { WAITV_BAR(16); LS(); } { WAITV_BAR(0); LS(); } { BAR(); LS(); }
    }
    {
      Tile R0, R1, R2;
      tile_init(R0); tile_init(R1); tile_init(R2);
      u64 seen = 0;
      const GAS u64* myflag = flags + (rb * 64 + lane);
      auto gate = [&](int p0, int p1) {
        if (hb) return;
        const u64 need = (0x0101010101010101ull << (smap(2 * p0) >> 1)) | (0x0101010101010101ull << (smap(2 * p1) >> 1));
#pragma unroll 1
        for (unsigned spins = 0; (seen & need) != need && spins < SPIN_CAP; ++spins) {
          seen = __ballot(__hip_atomic_load(myflag, RLX_AGENT) == tag);
          if ((seen & need) != need) __builtin_amdgcn_s_sleep(1);
        }
      };
      auto w_ld4 = [&](Tile& R, int st, int q) {
        const unsigned s0 = (unsigned)smap(st) * (64u * WROW);
#pragma unroll
        for (int p = 4 * q; p < 4 * q + 4; ++p) ld16<0, 0>(R.v[p], rwo, vow, s0 + (p >> 3) * (1024u * WROW) + (p & 7) * (8u * WROW));
      };
#pragma unroll
      for (int t = 0; t < 4; ++t) { BAR(); LS(); w_ld4(R0, 5, t); }
#pragma unroll
      for (int t = 0; t < 4; ++t) { BAR(); LS(); w_ld4(R1, 6, t); }
#pragma unroll
      for (int t = 0; t < 4; ++t) { BAR(); LS(); w_ld4(R2, 7, t); }
#pragma unroll 1
      for (int t = 12; t <= 15; ++t) { BAR(); LS(); }
      gate(0, 1); { BAR(); LS(); }
      gate(2, 2); { BAR(); LS(); } tile_wr<0>(R0, wdst + 4 * SLOT);
      { BAR_L(); LS(); } tile_wr<0>(R1, wdst);
      gate(3, 3); { BAR_L(); LS(); } tile_wr<0>(R2, wdst + SLOT);
#pragma unroll 1
      for (int p = 4; p <= 7; ++p) { { BAR_L(); LS(); } gate(p, p); { BAR(); LS(); } }
#pragma unroll 1
      for (int t = 11; t <= 16; ++t) { BAR(); LS(); }
    }
    if (wg == 0 && wid == 4) {
#pragma unroll 1
      for (unsigned spins = 0; spins < SPIN_CAP; ++spins) {
        bool ok = true;
#pragma unroll
        for (int i = 0; i < 16; ++i) ok &= __hip_atomic_load(flags + (lane + 64 * i), RLX_AGENT) == tag;
        if (__all(ok)) break;
        __builtin_amdgcn_s_sleep(2);
      }
      if (lane == 0) __hip_atomic_store((GAS u64*)(ws + WS_EPOCH), tag, RLX_AGENT);
    }
    return;
  }
  STAMP(0);
  const u64 tag = __hip_atomic_load((GAS u64*)(ws + WS_EPOCH), RLX_AGENT) + EPOCH_INC;
  const int lw = wid;
  const int r = lane & 31, hh = lane >> 5, qb = wid;
  const int tq = (lane >> 2) & 3, tp = lane & 3, blk = (lane >> 4) & 1;
  const LAS char* L = (const LAS char*)lds;
  const int rowe = 64 * (r & 7) + 16 * (hh ^ ((r >> 2) & 3));
  const int kbase_e = 2048 * (r >> 3) + rowe, kbase_o = kbase_e ^ 32;
  bf16x8 qf[8];
  bf16x8 kf[16], vf[16];
#define KRD(i, SP) kf[i] = rd128((SP) + ((((i) >> 1) & 1) ? kbase_o : kbase_e) + 512 * ((i) >> 2) + 8192 * ((i) & 1))
  {
    const int xbase_e = 1024 * (4 * qb + (r >> 3)) + rowe, xbase_o = xbase_e ^ 32;
    const int pp = (tp == 1) ? 2 : (tp == 2) ? 1 : tp;
    const int wbase = 16384 + hh * 2048 + tq * 64 + blk * 32 + pp * 8;
    f32x16 acc[4] = {};
    bf16x8 xf[4], wf[16];
    int sl = 2 * SLOT;
#define WRD(i, SP) wf[i] = rdtr((SP) + wbase + 4096 * ((i) >> 2) + 512 * ((i) & 3), (SP) + wbase + 4096 * ((i) >> 2) + 512 * ((i) & 3) + 256)
#define XRD(ks, SP) xf[ks] = rd128((SP) + (((ks) & 1) ? xbase_o : xbase_e) + 512 * ((ks) >> 1))
#define GQ(g, BODY) do { acc[(g) & 3] = MFMA(wf[g], xf[(g) >> 2], acc[(g) & 3]); BODY; SBAR(); } while (0)
    asm volatile("" : "+s"(out));
    __builtin_amdgcn_s_waitcnt(0xC07F);
    BAR();
    STAMP(1);
    { const LAS char* S = L + sl; XRD(0, S); WRD(0, S); SBAR(); WRD(1, S); WRD(2, S); SBAR(); WRD(3, S); XRD(1, S); SBAR(); WRD(4, S); SBAR(); WRD(5, S); SBAR(); }
#define QCHUNK(LAST) do { const LAS char* S = L + sl; sl = sl == 2 * SLOT ? 0 : sl + SLOT; const LAS char* N = (LAST) ? L + 3 * SLOT : L + sl; SBAR(); \
      GQ(0, { WRD(6, S); XRD(2, S); }); GQ(1, { WRD(7, S); XRD(3, S); }); GQ(2, { WRD(8, S); }); GQ(3, { WRD(9, S); WRD(10, S); }); GQ(4, WRD(11, S)); GQ(5, { WRD(12, S); WRD(13, S); }); \
      GQ(6, { WRD(14, S); }); GQ(7, WRD(15, S)); GQ(8, {}); GQ(9, {}); GQ(10, {}); \
      BAR_L(); SBAR(); GQ(11, {}); \
      if (!(LAST)) { XRD(0, N); WRD(0, N); SBAR(); GQ(12, { WRD(1, N); WRD(2, N); }); GQ(13, { WRD(3, N); XRD(1, N); }); GQ(14, { WRD(4, N); }); GQ(15, WRD(5, N)); } \
      else { GQ(12, { KRD(0, N); KRD(1, N); }); GQ(13, { KRD(2, N); KRD(3, N); }); GQ(14, { KRD(4, N); KRD(5, N); }); GQ(15, { KRD(6, N); KRD(7, N); }); } } while (0)
#pragma unroll 1
    for (int c = 0; c < 15; ++c) {
      QCHUNK(false);
    }
    QCHUNK(true);
#pragma unroll
    for (int d = 0; d < 4; ++d)
#pragma unroll
      for (int i = 0; i < 16; ++i) acc[d][i] *= C2;
#pragma unroll
    for (int s = 0; s < 8; ++s) qf[s] = pack8(acc[s >> 1], s & 1);
  }
  STAMP(2);
  f32x16 o[4] = {};
  {
    const int vbase = 16384 + hh * 256 + tq * 64 + blk * 32 + tp * 8;
    float l = 0.f;
    f32x16 sA0, sA1, sB0, sB1, negm;
    u32x4 pw[4];
    const f32x16 zero16 = {};
    int sk = 3 * SLOT;
    int sv = 3 * SLOT;
#define VRD(i, SP) vf[i] = rdtr((SP) + vbase + 4096 * ((i) >> 2) + 512 * ((i) & 3), (SP) + vbase + 4096 * ((i) >> 2) + 512 * ((i) & 3) + 2048)
#define EX(v) __builtin_amdgcn_exp2f(v)
#define PF(k) __builtin_bit_cast(bf16x8, pw[k])
#define LW(n) __builtin_amdgcn_s_waitcnt(0xC07F | ((n) << 8))
    {
      const LAS char* KP = L + sk;
#pragma unroll
      for (int i = 8; i < 16; ++i) KRD(i, KP);
      sA0 = MFMA(kf[0], qf[0], zero16); sA1 = MFMA(kf[1], qf[0], zero16);
#pragma unroll
      for (int s = 1; s < 8; ++s) { sA0 = MFMA(kf[2 * s], qf[s], sA0); sA1 = MFMA(kf[2 * s + 1], qf[s], sA1); }
      BAR_L();
      STAMP(3);
      sk = nxt5(sk);
      const LAS char* KN = L + sk;
      KRD(0, KN); SBAR(); KRD(1, KN); SBAR(); KRD(2, KN); SBAR(); KRD(3, KN); SBAR();
      float a = MX3(sA0[0], sA0[1], sA1[0]); a = MX3(a, sA1[1], sA0[2]);
#pragma unroll
      for (int i = 3; i < 16; i += 2) a = MX3(a, sA0[i], (i + 1 < 16) ? sA0[i + 1] : sA0[i]);
#pragma unroll
      for (int i = 2; i < 16; i += 2) a = MX3(a, sA1[i], sA1[i + 1]);
      const float m = both_max(a);
#pragma unroll
      for (int i = 0; i < 16; ++i) { sA0[i] -= m; sA1[i] -= m; negm[i] = -m; }
#pragma unroll
      for (int i = 0; i < 13; ++i) sA0[i] = EX(sA0[i]);
    }
#define GA(i, X, BODY) do { if ((i) < 2) mfma_new(X, kf[i], qf[0], negm); else mfma_acc(X, kf[i], qf[(i) >> 1]); if ((i) + 4 < 16) { KRD((i) + 4, KP_); } else { VRD((i) - 12, VP_); } BODY; } while (0)
#define E1(Y, e) do { Y[e] = aexp(Y[e]); } while (0)
#define E2(Y, e) do { Y[e] = aexp(Y[e]); Y[e + 1] = aexp(Y[e + 1]); } while (0)
#define S1(Y, e) aadd1(l, Y[e])
#define S2(Y, e) aadd2(l, Y[e], Y[e + 1])
#define S3(Y, e) aadd3(l, Y[e], Y[e + 1], Y[e + 2])
#define S4(Y, e) aadd4(l, Y[e], Y[e + 1], Y[e + 2], Y[e + 3])
#define PK(k, d, Y, e) do { pw[k][d] = acvt(Y[e], Y[e + 1]); } while (0)
#define PHASE_A(X0, X1, Y0, Y1) do { const LAS char* KP_ = L + sk; const LAS char* VP_ = L + sv; \
      LW(2); GA(0,  X0, { E2(Y0, 13); }); \
             GA(1,  X1, { PK(0, 0, Y0, 0); E1(Y0, 15); E1(Y1, 0); }); \
      LW(2); GA(2,  X0, { E2(Y1, 1); }); \
             GA(3,  X1, { PK(0, 1, Y0, 2); E2(Y1, 3); }); \
      LW(2); GA(4,  X0, { E2(Y1, 5); }); \
             GA(5,  X1, { PK(0, 2, Y0, 4); E2(Y1, 7); }); \
      LW(2); GA(6,  X0, { E2(Y1, 9); }); \
             GA(7,  X1, { PK(0, 3, Y0, 6); E2(Y1, 11); }); \
      LW(2); GA(8,  X0, { E2(Y1, 13); }); \
             GA(9,  X1, { PK(1, 0, Y0, 8); S2(Y0, 0); E1(Y1, 15); }); \
      LW(2); GA(10, X0, { PK(1, 1, Y0, 10); S3(Y0, 2); }); \
             GA(11, X1, { PK(1, 2, Y0, 12); S4(Y0, 5); }); \
      LW(2); GA(12, X0, { PK(1, 3, Y0, 14); S2(Y0, 9); }); \
             GA(13, X1, { S4(Y0, 11); }); \
      LW(4); GA(14, X0, { aadd3(l, Y0[15], Y1[0], Y1[1]); }); \
             GA(15, X1, { S2(Y1, 2); }); \
      } while (0)
#define GB(i, NEXTK, BODY) do { mfma_acc(o[(i) & 3], vf[i], PF((i) >> 2)); if ((i) + 4 < 16) { VRD((i) + 4, VP_); } else if (NEXTK) { KRD((i) - 12, KN_); } BODY; } while (0)
#define PHASE_B(X0, X1, Y1, NEXTK) do { const LAS char* VP_ = L + sv; const LAS char* KN_ = L + sk; float a_; float alpha_ = 1.f; bool resc_ = false; \
      LW(4); GB(0, NEXTK, { PK(2, 0, Y1, 0); S2(Y1, 4); }); \
             GB(1, NEXTK, { PK(2, 1, Y1, 2); PK(2, 2, Y1, 4); PK(2, 3, Y1, 6); S1(Y1, 6); }); \
      LW(4); GB(2, NEXTK, { a_ = amax7(X0[0], X0[1], X0[2], X0[3], X0[4], X0[5], X0[6]); }); \
             GB(3, NEXTK, { amax8(a_, X0[7], X0[8], X0[9], X0[10], X0[11], X0[12], X0[13], X0[14]); }); \
      LW(4); GB(4, NEXTK, { amax6(a_, X0[15], X1[0], X1[1], X1[2], X1[3], X1[4]); }); \
             GB(5, NEXTK, { amax8(a_, X1[5], X1[6], X1[7], X1[8], X1[9], X1[10], X1[11], X1[12]); }); \
      LW(4); GB(6, NEXTK, { amax3z(a_, X1[13], X1[14], X1[15]); S1(Y1, 7); }); \
             GB(7, NEXTK, { S2(Y1, 8); }); \
      if (__builtin_expect(__any(a_ > THR), 0)) { const float dl_ = both_max(a_); alpha_ = __builtin_amdgcn_exp2f(-dl_); resc_ = true; \
        _Pragma("unroll") for (int i_ = 0; i_ < 16; ++i_) { X0[i_] -= dl_; X1[i_] -= dl_; negm[i_] -= dl_; } } \
      LW(4); GB(8,  NEXTK, { PK(3, 0, Y1, 8); PK(3, 1, Y1, 10); S1(Y1, 10); }); \
             GB(9,  NEXTK, { PK(3, 2, Y1, 12); PK(3, 3, Y1, 14); E1(X0, 0); }); \
      LW(4); GB(10, NEXTK, { S1(Y1, 11); E1(X0, 1); }); \
             GB(11, NEXTK, { E2(X0, 2); }); \
      LW(4); GB(12, NEXTK, { S1(Y1, 12); E2(X0, 4); }); \
             GB(13, NEXTK, { S1(Y1, 13); E2(X0, 6); }); \
      if (NEXTK) LW(2); else LW(0); \
             GB(14, NEXTK, { S1(Y1, 14); E2(X0, 8); }); \
             GB(15, NEXTK, { S1(Y1, 15); E2(X0, 10); E1(X0, 12); }); \
      if (__builtin_expect(resc_, 0)) { asm volatile("s_nop 7\n\ts_nop 7" : "+v"(o[0]), "+v"(o[1]), "+v"(o[2]), "+v"(o[3]) :: "memory"); \
        l *= alpha_; _Pragma("unroll") for (int d_ = 0; d_ < 4; ++d_) _Pragma("unroll") for (int i_ = 0; i_ < 16; ++i_) o[d_][i_] *= alpha_; } \
      sv = nxt5(sv); } while (0)
    asm volatile("s_nop 1" : "+v"(negm), "+v"(sA0), "+v"(sA1) :: "memory");
#pragma unroll 1
    for (int t = 1; t < 15; t += 2) {
      PHASE_A(sB0, sB1, sA0, sA1); BAR(); sk = nxt5(sk); PHASE_B(sB0, sB1, sA1, true);
      PHASE_A(sA0, sA1, sB0, sB1); BAR(); sk = nxt5(sk); PHASE_B(sA0, sA1, sB1, true);
    }
    PHASE_A(sB0, sB1, sA0, sA1); BAR(); PHASE_B(sB0, sB1, sA1, false);
    {
      const LAS char* VP_ = L + sv;
#pragma unroll
      for (int i = 0; i < 16; ++i) VRD(i, VP_);
#pragma unroll
      for (int i = 13; i < 16; ++i) sB0[i] = EX(sB0[i]);
      const bf16x8 p0 = pack8(sB0, 0), p1 = pack8(sB0, 1);
#pragma unroll
      for (int db = 0; db < 4; ++db) o[db] = MFMA(vf[db], p0, o[db]);
#pragma unroll
      for (int i = 0; i < 16; ++i) sB1[i] = EX(sB1[i]);
#pragma unroll
      for (int db = 0; db < 4; ++db) o[db] = MFMA(vf[4 + db], p1, o[db]);
      const bf16x8 p2 = pack8(sB1, 0), p3 = pack8(sB1, 1);
#pragma unroll
      for (int db = 0; db < 4; ++db) o[db] = MFMA(vf[8 + db], p2, o[db]);
#pragma unroll
      for (int i = 0; i < 16; ++i) l += sB0[i];
#pragma unroll
      for (int db = 0; db < 4; ++db) o[db] = MFMA(vf[12 + db], p3, o[db]);
#pragma unroll
      for (int i = 0; i < 16; ++i) l += sB1[i];
      l = both_sum(l);
    }
    STAMP(7);
    const float inv = 1.0f / l;
    const unsigned obase = (unsigned)(((b * 4 + qb) * 256 + h * 8) * 1024);
    const __amdgpu_buffer_rsrc_t rs = __builtin_amdgcn_make_buffer_rsrc((void*)(ws + obase), 0, 8192, 0x00020000);
#pragma unroll
    for (int db = 0; db < 4; ++db) {
#pragma unroll
      for (int i = 0; i < 16; ++i) o[db][i] *= inv;
#pragma unroll
      for (int s = 0; s < 2; ++s) __builtin_amdgcn_raw_buffer_store_b128(__builtin_bit_cast(u32x4, pack8(o[db], s)), rs, lane * 16 + (db * 2 + s) * 1024, 0, OST_AUX);
    }
    asm volatile("s_waitcnt vmcnt(0)" ::: "memory");
    if (lane == 0) __hip_atomic_store(flags + ((b * 4 + qb) * 32 + h), tag, RLX_AGENT);
  }
  STAMP(8);
  {
    BAR_L();
    STAMP(9);
    const __amdgpu_buffer_rsrc_t rs = __builtin_amdgcn_make_buffer_rsrc((void*)ws, 0, 8 << 20, 0x00020000);
    const unsigned ob0 = (unsigned)((rb * 2 * 256 + lw * 64) * 1024);
    const int abase = lw * 8192 + hh * 256 + tq * 64 + blk * 32 + tp * 8;
    f32x16 acc[2][2] = {};
    u32x4 bq[4][8];
    bf16x8 af[8];
#define OLD(buf, st) do { const unsigned so_ = ob0 + (unsigned)(smap(st) * 4096); _Pragma("unroll") for (int i_ = 0; i_ < 8; ++i_) \
      bq[buf][i_] = __builtin_amdgcn_raw_buffer_load_b128(rs, lane * 16 + (i_ >> 1) * 1024, so_ + (i_ & 1) * 262144u, OLD_AUX); } while (0)
#define ARD(i_, SP) af[i_] = rdtr((SP) + 2048 * ((i_) >> 1) + 512 * ((i_) & 1), (SP) + 2048 * ((i_) >> 1) + 512 * ((i_) & 1) + 1024)
#define GO(buf, i_, BODY) do { acc[(i_) & 1][0] = MFMA(af[i_], __builtin_bit_cast(bf16x8, bq[buf][((i_) >> 1) * 2]), acc[(i_) & 1][0]); \
        acc[(i_) & 1][1] = MFMA(af[i_], __builtin_bit_cast(bf16x8, bq[buf][((i_) >> 1) * 2 + 1]), acc[(i_) & 1][1]); BODY; SBAR(); } while (0)
#define OST(buf, LAST) do { const LAS char* S_ = L + sw + abase; sw = nxt5(sw); const LAS char* N_ = L + sw + abase; SBAR(); \
      GO(buf, 0, ARD(4, S_)); GO(buf, 1, ARD(5, S_)); GO(buf, 2, ARD(6, S_)); GO(buf, 3, ARD(7, S_)); GO(buf, 4, {}); GO(buf, 5, {}); \
      BAR_L(); \
      GO(buf, 6, if (!(LAST)) { ARD(0, N_); ARD(1, N_); }); GO(buf, 7, if (!(LAST)) { ARD(2, N_); ARD(3, N_); }); } while (0)
    int sw = 4 * SLOT;
    OLD(0, 0); OLD(1, 1); OLD(2, 2); OLD(3, 3);
    { const LAS char* S_ = L + sw + abase; ARD(0, S_); ARD(1, S_); ARD(2, S_); ARD(3, S_); }
#pragma unroll 1
    for (int st = 0; st < 12; st += 4) {
      OST(0, false); OLD(0, st + 4);
      OST(1, false); OLD(1, st + 5);
      OST(2, false); OLD(2, st + 6);
      OST(3, false); OLD(3, st + 7);
    }
    OST(0, false); OST(1, false); OST(2, false); OST(3, true);
    STAMP(10);
    LAS char* W = (LAS char*)lds;
#pragma unroll
    for (int ti = 0; ti < 4; ++ti) {
      const f32x16& a = acc[ti & 1][ti >> 1];
#pragma unroll
      for (int g = 0; g < 4; ++g) { f32x4 v = {a[4 * g], a[4 * g + 1], a[4 * g + 2], a[4 * g + 3]}; *(LAS f32x4*)(W + (ti * 4 + lw) * 4096 + g * 1024 + lane * 16) = v; }
    }
    BAR_L();
    f32x4 sum[4];
#pragma unroll
    for (int g = 0; g < 4; ++g) {
      sum[g] = *(const LAS f32x4*)(L + (lw * 4 + 0) * 4096 + g * 1024 + lane * 16);
#pragma unroll
      for (int s = 1; s < 4; ++s) sum[g] += *(const LAS f32x4*)(L + (lw * 4 + s) * 4096 + g * 1024 + lane * 16);
    }
    const int nb = lw & 1, q2 = lw >> 1;
    const int row = rb * 64 + q2 * 32 + r;
    char* orow = out + (size_t)row * DM * 2 + (cb * 64 + nb * 32) * 2;
#pragma unroll
    for (int g = 0; g < 4; g += 2) {
      unsigned a0 = cvtpk(sum[g][0], sum[g][1]), a1 = cvtpk(sum[g][2], sum[g][3]);
      unsigned b0 = cvtpk(sum[g + 1][0], sum[g + 1][1]), b1 = cvtpk(sum[g + 1][2], sum[g + 1][3]);
      auto r0 = __builtin_amdgcn_permlane32_swap(a0, b0, false, false);
      auto r1 = __builtin_amdgcn_permlane32_swap(a1, b1, false, false);
      u32x4 w = {r0[0], r1[0], r0[1], r1[1]};
      *(GAS u32x4*)(orow + g * 16 + hh * 16) = w;
    }
    STAMP(11);
  }
}

extern "C" void kernel_launch(void* const* d_in, const int* in_sizes, int n_in, void* d_out, int out_size, void* d_ws, size_t ws_size, hipStream_t stream) {
  mha_fused<<<256, 512, 0, stream>>>((const char*)d_in[0], (const char*)d_in[1], (const char*)d_in[2], (const char*)d_in[3], (const char*)d_in[4], (char*)d_out, (char*)d_ws);
}
```

```cpp
#include <hip/hip_runtime.h>
#include <hip/hip_bf16.h>

#define KV_B 2
#define CODE_KB 24

typedef __bf16 bf16x8 __attribute__((ext_vector_type(8)));
typedef __bf16 bf16x2 __attribute__((ext_vector_type(2)));
typedef short s16x4 __attribute__((ext_vector_type(4)));
typedef short s16x8 __attribute__((ext_vector_type(8)));
typedef float f32x16 __attribute__((ext_vector_type(16)));
typedef float f32x4 __attribute__((ext_vector_type(4)));
typedef float f32x2 __attribute__((ext_vector_type(2)));
typedef unsigned u32x4 __attribute__((ext_vector_type(4)));
typedef unsigned long long u64;
#define LAS __attribute__((address_space(3)))
#define GAS __attribute__((address_space(1)))
#define DI __device__ __forceinline__
#define MFMA(a, b, c) __builtin_amdgcn_mfma_f32_32x32x16_bf16((a), (b), (c), 0, 0, 0)
#define SBAR() __builtin_amdgcn_sched_barrier(0)
#define RLX_AGENT __ATOMIC_RELAXED, __HIP_MEMORY_SCOPE_AGENT
#define SRD(p, n) __builtin_amdgcn_make_buffer_rsrc((void*)(p), 0, (n), 0x00020000)

constexpr int SQ = 128, DM = 1024, NH = 32, SK = 1024, DH = 128;
constexpr int SLOT = 32768, LDS_BYTES = 163840;
constexpr float C2 = 0.08838834764831845f * 1.4426950408889634f;
constexpr float THR = 8.0f;
constexpr unsigned WS_FLAGS = 8u << 20, WS_EPOCH = WS_FLAGS + 65536u, WS_FLAGS2 = 9u << 20, WS_PART = 16u << 20;
constexpr u64 EPOCH_INC = 0x9E3779B97F4A7C15ull;
constexpr unsigned SPIN_CAP = 1u << 22;
constexpr int SC1 = 16, NT = 2;

#define STAMP(i) do {} while (0)
#define LS() do {} while (0)

DI unsigned rfl(unsigned x) { return __builtin_amdgcn_readfirstlane(x); }
DI unsigned cvtpk(float lo, float hi) { f32x2 v = {lo, hi}; bf16x2 b = __builtin_convertvector(v, bf16x2); return __builtin_bit_cast(unsigned, b); }
DI bf16x8 pack8(const f32x16& x, int s) {
  u32x4 w = {cvtpk(x[8 * s + 0], x[8 * s + 1]), cvtpk(x[8 * s + 2], x[8 * s + 3]), cvtpk(x[8 * s + 4], x[8 * s + 5]), cvtpk(x[8 * s + 6], x[8 * s + 7])};
  return __builtin_bit_cast(bf16x8, w);
}
DI void dma16(__amdgpu_buffer_rsrc_t rs, unsigned voff, unsigned soff, unsigned ldsaddr) {
  asm volatile("s_mov_b32 m0, %0\n\ts_nop 0\n\tbuffer_load_dwordx4 %1, %2, %3 offen lds" ::"s"(ldsaddr), "v"(voff), "s"(rs), "s"(soff) : "memory");
}
#define DMA_PC(V, NTS) "buffer_load_dwordx4 " V ", %4, %0 offen " NTS "lds\n\t"
#define DMA_2(VA, VB, NTS) DMA_PC(VA, NTS) "s_add_u32 m0, m0, 0x400\n\ts_add_u32 %0, %0, %5\n\t" DMA_PC(VB, NTS) "s_add_u32 m0, m0, 0x400\n\ts_add_u32 %0, %0, %6\n\t"
template <int SEL> DI void dma_tile(__amdgpu_buffer_rsrc_t rs, unsigned voe, unsigned voo, unsigned soff, unsigned p1, unsigned p2, unsigned ldsaddr) {
  const unsigned d2 = p2 - p1;
  if constexpr (SEL == 0)
    asm volatile("s_mov_b32 m0, %1\n\ts_nop 0\n\t" DMA_2("%2", "%3", "") DMA_2("%2", "%3", "") DMA_2("%2", "%3", "") DMA_2("%2", "%3", "") DMA_2("%2", "%3", "") DMA_2("%2", "%3", "") DMA_2("%2", "%3", "") DMA_2("%2", "%3", "")
                 : "+&s"(soff) : "s"(ldsaddr), "v"(voe), "v"(voo), "s"(rs), "s"(p1), "s"(d2) : "memory", "scc");
  else
    asm volatile("s_mov_b32 m0, %1\n\ts_nop 0\n\t" DMA_2("%2", "%2", "nt ") DMA_2("%3", "%3", "nt ") DMA_2("%2", "%2", "nt ") DMA_2("%3", "%3", "nt ") DMA_2("%2", "%2", "nt ") DMA_2("%3", "%3", "nt ") DMA_2("%2", "%2", "nt ") DMA_2("%3", "%3", "nt ")
                 : "+&s"(soff) : "s"(ldsaddr), "v"(voe), "v"(voo), "s"(rs), "s"(p1), "s"(d2) : "memory", "scc");
}
DI bf16x8 rd128(const LAS char* p) { return *(const LAS bf16x8*)p; }
DI bf16x8 rdtr(const LAS char* lo, const LAS char* up) {
  s16x4 a = __builtin_amdgcn_ds_read_tr16_b64_v4i16((LAS s16x4*)lo);
  s16x4 b = __builtin_amdgcn_ds_read_tr16_b64_v4i16((LAS s16x4*)up);
  s16x8 c = __builtin_shufflevector(a, b, 0, 1, 2, 3, 4, 5, 6, 7);
  return __builtin_bit_cast(bf16x8, c);
}
#define BAR() asm volatile("s_barrier" ::: "memory")
#define BAR_L() asm volatile("s_waitcnt lgkmcnt(0)\n\ts_barrier" ::: "memory")
#define WAITV(N) asm volatile("s_waitcnt vmcnt(" #N ")" ::: "memory")
#define WAITV_BAR(N) asm volatile("s_waitcnt vmcnt(" #N ")\n\ts_barrier" ::: "memory")
#define MX3(a, b, c) __builtin_fmaxf(__builtin_fmaxf((a), (b)), (c))
DI float both_max(float x) { auto r = __builtin_amdgcn_permlane32_swap(__float_as_uint(x), __float_as_uint(x), false, false); return __builtin_fmaxf(__uint_as_float(r[0]), __uint_as_float(r[1])); }
DI float both_sum(float x) { auto r = __builtin_amdgcn_permlane32_swap(__float_as_uint(x), __float_as_uint(x), false, false); return __uint_as_float(r[0]) + __uint_as_float(r[1]); }
DI void mfma_acc(f32x16& d, const bf16x8& a, const bf16x8& b) { asm volatile("v_mfma_f32_32x32x16_bf16 %0, %1, %2, %0" : "+v"(d) : "v"(a), "v"(b) : "memory"); }
DI void mfma_new(f32x16& d, const bf16x8& a, const bf16x8& b, const f32x16& c) { asm volatile("v_mfma_f32_32x32x16_bf16 %0, %1, %2, %3" : "=&v"(d) : "v"(a), "v"(b), "v"(c) : "memory"); }
DI float aexp(float x) { asm volatile("v_exp_f32 %0, %0" : "+v"(x) :: "memory"); return x; }
DI unsigned acvt(float a, float b) { unsigned r; asm volatile("v_cvt_pk_bf16_f32 %0, %1, %2" : "=v"(r) : "v"(a), "v"(b) : "memory"); return r; }
DI void aadd1(float& l, float a) { asm volatile("v_add_f32 %0, %0, %1" : "+v"(l) : "v"(a) : "memory"); }
DI void aadd2(float& l, float a, float b) { asm volatile("v_add_f32 %0, %0, %1\n\tv_add_f32 %0, %0, %2" : "+&v"(l) : "v"(a), "v"(b) : "memory"); }
DI void aadd3(float& l, float a, float b, float c) { asm volatile("v_add_f32 %0, %0, %1\n\tv_add_f32 %0, %0, %2\n\tv_add_f32 %0, %0, %3" : "+&v"(l) : "v"(a), "v"(b), "v"(c) : "memory"); }
DI void aadd4(float& l, float a, float b, float c, float d) { asm volatile("v_add_f32 %0, %0, %1\n\tv_add_f32 %0, %0, %2\n\tv_add_f32 %0, %0, %3\n\tv_add_f32 %0, %0, %4" : "+&v"(l) : "v"(a), "v"(b), "v"(c), "v"(d) : "memory"); }
DI float amax7(float a, float b, float c, float d, float e, float f, float g) { float r; asm volatile("v_max3_f32 %0, %1, %2, %3\n\tv_max3_f32 %0, %0, %4, %5\n\tv_max3_f32 %0, %0, %6, %7" : "=&v"(r) : "v"(a), "v"(b), "v"(c), "v"(d), "v"(e), "v"(f), "v"(g) : "memory"); return r; }
DI void amax6(float& r, float a, float b, float c, float d, float e, float f) { asm volatile("v_max3_f32 %0, %0, %1, %2\n\tv_max3_f32 %0, %0, %3, %4\n\tv_max3_f32 %0, %0, %5, %6" : "+&v"(r) : "v"(a), "v"(b), "v"(c), "v"(d), "v"(e), "v"(f) : "memory"); }
DI void amax8(float& r, float a, float b, float c, float d, float e, float f, float g, float h) { asm volatile("v_max3_f32 %0, %0, %1, %2\n\tv_max3_f32 %0, %0, %3, %4\n\tv_max3_f32 %0, %0, %5, %6\n\tv_max3_f32 %0, %0, %7, %8" : "+&v"(r) : "v"(a), "v"(b), "v"(c), "v"(d), "v"(e), "v"(f), "v"(g), "v"(h) : "memory"); }
DI void amax3z(float& r, float a, float b, float c) { asm volatile("v_max3_f32 %0, %0, %1, %2\n\tv_max3_f32 %0, %0, %3, 0" : "+&v"(r) : "v"(a), "v"(b), "v"(c) : "memory"); }
DI int nxt5(int s) { return s == 4 * SLOT ? 0 : s + SLOT; }
struct Tile { u32x4 v[16]; };
DI void tile_init(Tile& t) {
#pragma unroll
  for (int p = 0; p < 16; ++p) asm volatile("" : "=v"(t.v[p]));
}
template <int AUX, int IMM> DI void ld16(u32x4& d, __amdgpu_buffer_rsrc_t rs, unsigned voff, unsigned soff) {
  if constexpr (AUX == NT) asm volatile("buffer_load_dwordx4 %0, %1, %2, %3 offen offset:%4 nt" : "+v"(d) : "v"(voff), "s"(rs), "s"(soff), "i"(IMM) : "memory");
  else                     asm volatile("buffer_load_dwordx4 %0, %1, %2, %3 offen offset:%4" : "+v"(d) : "v"(voff), "s"(rs), "s"(soff), "i"(IMM) : "memory");
}
template <int P> DI void wr16(const u32x4& d, unsigned addr) { asm volatile("ds_write_b128 %0, %1 offset:%2" ::"v"(addr), "v"(d), "i"(P * 1024) : "memory"); }
DI void tile_wr(const Tile& t, unsigned addr) {
  wr16<0>(t.v[0], addr); wr16<1>(t.v[1], addr); wr16<2>(t.v[2], addr); wr16<3>(t.v[3], addr); wr16<4>(t.v[4], addr); wr16<5>(t.v[5], addr); wr16<6>(t.v[6], addr); wr16<7>(t.v[7], addr);
  wr16<8>(t.v[8], addr); wr16<9>(t.v[9], addr); wr16<10>(t.v[10], addr); wr16<11>(t.v[11], addr); wr16<12>(t.v[12], addr); wr16<13>(t.v[13], addr); wr16<14>(t.v[14], addr); wr16<15>(t.v[15], addr);
}

__global__ __launch_bounds__(512, 2) void mha_fused(const char* __restrict__ x, const char* __restrict__ kc, const char* __restrict__ vc,
                                                    const char* __restrict__ wq, const char* __restrict__ wo, char* out, char* __restrict__ ws) {
  __shared__ __attribute__((aligned(16))) char lds[LDS_BYTES];
  u64 pc0; asm volatile("s_getpc_b64 %0" : "=s"(pc0));
  const int tid = threadIdx.x, lane = tid & 63;
  const int wid = rfl(tid >> 6);
  const int wg = blockIdx.x;
  const int xcd = wg & 7, j = wg >> 3;
  const int b = j & 7, h = xcd * 4 + (j >> 3);
  const bool late = (j ^ (j >> 3)) & 1;
  const int kq = xcd >> 1, ob = (xcd & 1) * 4 + (j >> 3), nb = j & 7;
  auto hmap = [&](int p) { return 8 * kq + 2 * (p & 3) + ((ob & 1) ^ (p >> 2)); };
  const unsigned lds0 = (unsigned)(size_t)(LAS char*)lds;
  GAS u64* flags = (GAS u64*)(ws + WS_FLAGS);

  if (wid >= 4) {
    const unsigned r8 = (lane >> 2) & 7, sub = lane >> 5, c4 = lane & 3;
    const unsigned hb = wid & 1;
    const unsigned xe = hb ? c4 : (c4 ^ (r8 >> 2)), xo = hb ? c4 : (c4 ^ (2 + (r8 >> 2)));
    const unsigned ldh = lds0 + hb * 16384u;
    const __amdgpu_buffer_rsrc_t rcode = SRD(pc0 & ~255ull, CODE_KB * 1024);
    if (wid >= 6) {
      const __amdgpu_buffer_rsrc_t rs = SRD((hb ? vc : kc) + (size_t)(b * NH + h) * SK * DH * 2, SK * DH * 2);
      const unsigned voe = r8 * 256u + sub * 64u + 16u * xe, voo = r8 * 256u + sub * 64u + 16u * xo;
      const int krot = (j + 5 * xcd) & 15;
      auto tile_dma = [&](int t, int so) { dma_tile<1>(rs, voe, voo, ((t + krot) & 15) * 16384u, 128u, 2048u, ldh + so); };
      const __amdgpu_buffer_rsrc_t rwo = SRD(wo + nb * 256, NH * DH * DM * 2 - nb * 256);
      const unsigned vw0 = (hb * 64u + r8) * 2048u + 16u * c4 + 64u * ((kq + sub) & 3), vw1 = (hb * 64u + r8) * 2048u + 16u * c4 + 64u * ((kq + 2 + sub) & 3);
      auto w_dma = [&](int p, int so) { dma_tile<0>(rwo, vw0, vw1, (unsigned)hmap(p) * (128u * 2048u), 0u, 16384u, ldh + so); };
      dma16(rcode, lane * 16u, ((j & 15) * 2 + hb) * 1024u, ldh + 4 * SLOT);
      int sl = 3 * SLOT;
      int n0 = 5;
      if (late) {
        const unsigned wdst = ldh + lane * 16u;
        Tile R0, R1, R2;
        tile_init(R0); tile_init(R1); tile_init(R2);
        auto tile_ld = [&](Tile& R, int t) {
#pragma unroll
          for (int q = 0; q < 4; ++q) {
            const unsigned so = ((t + krot) & 15) * 16384u + q * 4096u;
            ld16<NT, 0>(R.v[4 * q], rs, voe, so); ld16<NT, 128>(R.v[4 * q + 1], rs, voe, so);
            ld16<NT, 2048>(R.v[4 * q + 2], rs, voo, so); ld16<NT, 2048 + 128>(R.v[4 * q + 3], rs, voo, so);
          }
        };
#pragma unroll 1
        for (int c = -1; c <= KV_B; ++c) BAR();
        tile_dma(0, 3 * SLOT); tile_dma(1, 4 * SLOT); tile_ld(R0, 2);
        WAITV(32); tile_ld(R1, 3);
        WAITV(32); tile_ld(R2, 4);
#pragma unroll 1
        for (int c = KV_B + 1; c <= 14; ++c) BAR();
        WAITV_BAR(0);
        tile_wr(R0, wdst);
        BAR_L(); tile_wr(R1, wdst + SLOT);
        if (hb) BAR_L();
        tile_wr(R2, wdst + 2 * SLOT);
        asm volatile("s_waitcnt lgkmcnt(0)" ::: "memory");
      } else {
#pragma unroll 1
        for (int c = -1; c <= 12 + (int)hb; ++c) BAR();
        __builtin_amdgcn_s_sleep(6);
        tile_dma(0, sl); sl = nxt5(sl);
        n0 = 1;
      }
#pragma unroll 1
      for (int n = n0; n <= 15; ++n) { WAITV_BAR(32); tile_dma(n, sl); sl = nxt5(sl); }
      if (!hb) {
        WAITV_BAR(32); WAITV_BAR(16); w_dma(0, sl); sl = nxt5(sl);
        WAITV_BAR(16); w_dma(1, sl); sl = nxt5(sl);
        BAR(); w_dma(2, sl); sl = nxt5(sl);
      } else {
#pragma unroll 1
        for (int s = 0; s <= 2; ++s) { WAITV_BAR(32); w_dma(s, sl); sl = nxt5(sl); }
      }
      WAITV_BAR(32);
      WAITV(31);
      __builtin_amdgcn_s_sleep(8);
      w_dma(3, sl); sl = nxt5(sl); w_dma(4, sl);
#pragma unroll 1
      for (int p = 5; p < 8; ++p) { BAR(); WAITV_BAR(47); sl = nxt5(sl); w_dma(p, sl); }
      BAR(); WAITV_BAR(47); BAR(); WAITV_BAR(32); BAR(); WAITV_BAR(16); BAR(); WAITV_BAR(0);
      return;
    }
    const u64 tag = __hip_atomic_load((GAS u64*)(ws + WS_EPOCH), RLX_AGENT) + EPOCH_INC;
    {
      const int qrot = (2 * (j & 7) + 4 * (j >> 3)) & 15;
      const __amdgpu_buffer_rsrc_t rs = hb ? SRD(wq + h * DH * 2, DM * NH * DH * 2 - h * DH * 2) : SRD(x + (size_t)b * SQ * DM * 2, SQ * DM * 2);
      const unsigned rstr = hb ? 8192u : 2048u;
      const unsigned voe = r8 * rstr + sub * 64u + 16u * xe, voo = r8 * rstr + sub * 64u + 16u * xo;
      const unsigned cstr = hb ? 64u * 8192u : 128u;
      const unsigned p1 = hb ? 128u : 16384u, p2 = hb ? 65536u : 32768u;
      auto issue_q = [&](int c, int so) { dma_tile<0>(rs, voe, voo, ((c + qrot) & 15) * cstr, p1, p2, ldh + so); };
      issue_q(0, 2 * SLOT); issue_q(1, 0); issue_q(2, SLOT);
      WAITV_BAR(32); LS();
      int sl = 2 * SLOT;
#pragma unroll 1
      for (int c = 0; c <= 12; ++c) { WAITV_BAR(16); LS(); issue_q(c + 3, sl); sl = sl == 2 * SLOT ? 0 : sl + SLOT; }
      WAITV_BAR(16); LS(); WAITV_BAR(0); LS(); BAR(); LS();
    }
    {
      u64 seen = 0;
      const GAS u64* myflag = flags + (((ob * 4 + kq) * 8 + nb) * 32 + (lane & 31));
      auto gate = [&](u64 need) {
        if (hb) return;
#pragma unroll 1
        for (unsigned spins = 0; (seen & need) != need && spins < SPIN_CAP; ++spins) {
          seen = __ballot(__hip_atomic_load(myflag, RLX_AGENT) == tag);
          if ((seen & need) != need) __builtin_amdgcn_s_sleep(1);
        }
      };
      auto hbit = [&](int p) { return 0x01010101ull << (hmap(p) & 7); };
#pragma unroll 1
      for (int t = 0; t <= 10; ++t) { BAR(); LS(); }
      u32x4 d0; asm volatile("" : "=v"(d0));
      ld16<0, 0>(d0, rcode, lane * 16u, ((j >> 1) * 2 + hb) * 1024u);
#pragma unroll 1
      for (int t = 11; t <= 14; ++t) { BAR(); LS(); }
      WAITV_BAR(0); LS();
      asm volatile("" ::"v"(d0));
      gate(hbit(0) | hbit(1)); BAR(); LS();
#pragma unroll 1
      for (int m = 0; m < 7; ++m) { if (m < 6) gate(hbit(m + 2)); BAR(); BAR(); LS(); }
    }
    if (wg == 0 && wid == 4) {
#pragma unroll 1
      for (unsigned spins = 0; spins < SPIN_CAP; ++spins) {
        bool ok = true;
#pragma unroll
        for (int i = 0; i < 16; ++i) ok &= __hip_atomic_load(flags + ((2 * i + (lane >> 5)) * 256 + (lane & 31)), RLX_AGENT) == tag;
        if (__all(ok)) break;
        __builtin_amdgcn_s_sleep(2);
      }
      if (lane == 0) __hip_atomic_store((GAS u64*)(ws + WS_EPOCH), tag, RLX_AGENT);
    }
    return;
  }
  STAMP(0);
  const u64 tag = __hip_atomic_load((GAS u64*)(ws + WS_EPOCH), RLX_AGENT) + EPOCH_INC;
  const int r = lane & 31, hh = lane >> 5, qb = wid;
  const int tq = (lane >> 2) & 3, tp = lane & 3, blk = (lane >> 4) & 1;
  const LAS char* L = (const LAS char*)lds;
  const int rowe = 64 * (r & 7) + 16 * (hh ^ ((r >> 2) & 3));
  const int kbase_e = 2048 * (r >> 3) + rowe, kbase_o = kbase_e ^ 32;
  const int tbase = hh * 256 + tq * 64 + blk * 32 + tp * 8;
  bf16x8 qf[8];
  bf16x8 kf[16], vf[16];
  f32x16 acc[4] = {};
#define KRD(i, SP) kf[i] = rd128((SP) + ((((i) >> 1) & 1) ? kbase_o : kbase_e) + 512 * ((i) >> 2) + 8192 * ((i) & 1))
  {
    const int xbase_e = 1024 * (4 * qb + (r >> 3)) + rowe, xbase_o = xbase_e ^ 32;
    const int pp = (tp == 1) ? 2 : (tp == 2) ? 1 : tp;
    const int wbase = 16384 + hh * 2048 + tq * 64 + blk * 32 + pp * 8;
    bf16x8 xf[4], wf[16];
    int sl = 2 * SLOT;
#define WRD(i, SP) wf[i] = rdtr((SP) + wbase + 4096 * ((i) >> 2) + 512 * ((i) & 3), (SP) + wbase + 4096 * ((i) >> 2) + 512 * ((i) & 3) + 256)
#define XRD(ks, SP) xf[ks] = rd128((SP) + (((ks) & 1) ? xbase_o : xbase_e) + 512 * ((ks) >> 1))
#define GQ(g, BODY) do { acc[(g) & 3] = MFMA(wf[g], xf[(g) >> 2], acc[(g) & 3]); BODY; SBAR(); } while (0)
    asm volatile("" : "+s"(out));
    __builtin_amdgcn_s_waitcnt(0xC07F);
    BAR();
    STAMP(1);
    { const LAS char* S = L + sl; XRD(0, S); WRD(0, S); SBAR(); WRD(1, S); WRD(2, S); SBAR(); WRD(3, S); XRD(1, S); SBAR(); WRD(4, S); SBAR(); WRD(5, S); SBAR(); }
#define QCHUNK(LAST) do { const LAS char* S = L + sl; sl = sl == 2 * SLOT ? 0 : sl + SLOT; const LAS char* N = (LAST) ? L + 3 * SLOT : L + sl; SBAR(); \
      GQ(0, { WRD(6, S); XRD(2, S); }); GQ(1, { WRD(7, S); XRD(3, S); }); GQ(2, { WRD(8, S); }); GQ(3, { WRD(9, S); WRD(10, S); }); GQ(4, WRD(11, S)); GQ(5, { WRD(12, S); WRD(13, S); }); \
      GQ(6, { WRD(14, S); }); GQ(7, WRD(15, S)); GQ(8, {}); GQ(9, {}); GQ(10, {}); \
      BAR_L(); SBAR(); GQ(11, {}); \
      if (!(LAST)) { XRD(0, N); WRD(0, N); SBAR(); GQ(12, { WRD(1, N); WRD(2, N); }); GQ(13, { WRD(3, N); XRD(1, N); }); GQ(14, { WRD(4, N); }); GQ(15, WRD(5, N)); } \
      else { GQ(12, { KRD(0, N); KRD(1, N); }); GQ(13, { KRD(2, N); KRD(3, N); }); GQ(14, { KRD(4, N); KRD(5, N); }); GQ(15, { KRD(6, N); KRD(7, N); }); } } while (0)
#pragma unroll 1
    for (int c = 0; c < 15; ++c) QCHUNK(false);
    QCHUNK(true);
#pragma unroll
    for (int d = 0; d < 4; ++d)
#pragma unroll
      for (int i = 0; i < 16; ++i) acc[d][i] *= C2;
#pragma unroll
    for (int s = 0; s < 8; ++s) qf[s] = pack8(acc[s >> 1], s & 1);
  }
  STAMP(2);
  {
    f32x16 o[4] = {};
    float l = 0.f;
    f32x16 sA0, sA1, sB0, sB1, negm;
    u32x4 pw[4];
    const f32x16 zero16 = {};
    int sk = 3 * SLOT;
    int sv = 3 * SLOT + 16384;
#define VRD(i, SP) vf[i] = rdtr((SP) + tbase + 4096 * ((i) >> 2) + 512 * ((i) & 3), (SP) + tbase + 4096 * ((i) >> 2) + 512 * ((i) & 3) + 2048)
#define EX(v) __builtin_amdgcn_exp2f(v)
#define PF(k) __builtin_bit_cast(bf16x8, pw[k])
#define LW(n) __builtin_amdgcn_s_waitcnt(0xC07F | ((n) << 8))
    {
      const LAS char* KP = L + sk;
#pragma unroll
      for (int i = 8; i < 16; ++i) KRD(i, KP);
      sA0 = MFMA(kf[0], qf[0], zero16); sA1 = MFMA(kf[1], qf[0], zero16);
#pragma unroll
      for (int s = 1; s < 8; ++s) { sA0 = MFMA(kf[2 * s], qf[s], sA0); sA1 = MFMA(kf[2 * s + 1], qf[s], sA1); }
      BAR_L();
      STAMP(3);
      sk = nxt5(sk);
      const LAS char* KN = L + sk;
      KRD(0, KN); SBAR(); KRD(1, KN); SBAR(); KRD(2, KN); SBAR(); KRD(3, KN); SBAR();
      float a = MX3(sA0[0], sA0[1], sA1[0]); a = MX3(a, sA1[1], sA0[2]);
#pragma unroll
      for (int i = 3; i < 16; i += 2) a = MX3(a, sA0[i], (i + 1 < 16) ? sA0[i + 1] : sA0[i]);
#pragma unroll
      for (int i = 2; i < 16; i += 2) a = MX3(a, sA1[i], sA1[i + 1]);
      const float m = both_max(a);
#pragma unroll
      for (int i = 0; i < 16; ++i) { sA0[i] -= m; sA1[i] -= m; negm[i] = -m; }
#pragma unroll
      for (int i = 0; i < 13; ++i) sA0[i] = EX(sA0[i]);
    }
#define GA(i, X, BODY) do { if ((i) < 2) mfma_new(X, kf[i], qf[0], negm); else mfma_acc(X, kf[i], qf[(i) >> 1]); if ((i) + 4 < 16) { KRD(((i) + 4) & 15, KP_); } else { VRD((i) & 3, VP_); } BODY; } while (0)
#define E1(Y, e) do { Y[e] = aexp(Y[e]); } while (0)
#define E2(Y, e) do { Y[e] = aexp(Y[e]); Y[e + 1] = aexp(Y[e + 1]); } while (0)
#define S1(Y, e) aadd1(l, Y[e])
#define S2(Y, e) aadd2(l, Y[e], Y[e + 1])
#define S3(Y, e) aadd3(l, Y[e], Y[e + 1], Y[e + 2])
#define S4(Y, e) aadd4(l, Y[e], Y[e + 1], Y[e + 2], Y[e + 3])
#define PK(k, d, Y, e) do { pw[k][d] = acvt(Y[e], Y[e + 1]); } while (0)
#define PHASE_A(X0, X1, Y0, Y1) do { const LAS char* KP_ = L + sk; const LAS char* VP_ = L + sv; \
      LW(2); GA(0,  X0, { E2(Y0, 13); }); \
             GA(1,  X1, { PK(0, 0, Y0, 0); E1(Y0, 15); E1(Y1, 0); }); \
      LW(2); GA(2,  X0, { E2(Y1, 1); }); \
             GA(3,  X1, { PK(0, 1, Y0, 2); E2(Y1, 3); }); \
      LW(2); GA(4,  X0, { E2(Y1, 5); }); \
             GA(5,  X1, { PK(0, 2, Y0, 4); E2(Y1, 7); }); \
      LW(2); GA(6,  X0, { E2(Y1, 9); }); \
             GA(7,  X1, { PK(0, 3, Y0, 6); E2(Y1, 11); }); \
      LW(2); GA(8,  X0, { E2(Y1, 13); }); \
             GA(9,  X1, { PK(1, 0, Y0, 8); S2(Y0, 0); E1(Y1, 15); }); \
      LW(2); GA(10, X0, { PK(1, 1, Y0, 10); S3(Y0, 2); }); \
             GA(11, X1, { PK(1, 2, Y0, 12); S4(Y0, 5); }); \
      LW(2); GA(12, X0, { PK(1, 3, Y0, 14); S2(Y0, 9); }); \
             GA(13, X1, { S4(Y0, 11); }); \
      LW(4); GA(14, X0, { aadd3(l, Y0[15], Y1[0], Y1[1]); }); \
             GA(15, X1, { S2(Y1, 2); }); \
      } while (0)
#define GB(i, NEXTK, BODY) do { mfma_acc(o[(i) & 3], vf[i], PF((i) >> 2)); if ((i) + 4 < 16) { VRD(((i) + 4) & 15, VP_); } else if (NEXTK) { KRD((i) & 3, KN_); } BODY; } while (0)
#define PHASE_B(X0, X1, Y1, NEXTK) do { const LAS char* VP_ = L + sv; const LAS char* KN_ = L + sk; float a_; float alpha_ = 1.f; bool resc_ = false; \
      LW(4); GB(0, NEXTK, { PK(2, 0, Y1, 0); S2(Y1, 4); }); \
             GB(1, NEXTK, { PK(2, 1, Y1, 2); PK(2, 2, Y1, 4); PK(2, 3, Y1, 6); S1(Y1, 6); }); \
      LW(4); GB(2, NEXTK, { a_ = amax7(X0[0], X0[1], X0[2], X0[3], X0[4], X0[5], X0[6]); }); \
             GB(3, NEXTK, { amax8(a_, X0[7], X0[8], X0[9], X0[10], X0[11], X0[12], X0[13], X0[14]); }); \
      LW(4); GB(4, NEXTK, { amax6(a_, X0[15], X1[0], X1[1], X1[2], X1[3], X1[4]); }); \
             GB(5, NEXTK, { amax8(a_, X1[5], X1[6], X1[7], X1[8], X1[9], X1[10], X1[11], X1[12]); }); \
      LW(4); GB(6, NEXTK, { amax3z(a_, X1[13], X1[14], X1[15]); S1(Y1, 7); }); \
             GB(7, NEXTK, { S2(Y1, 8); }); \
      if (__builtin_expect(__any(a_ > THR), 0)) { const float dl_ = both_max(a_); alpha_ = __builtin_amdgcn_exp2f(-dl_); resc_ = true; \
        _Pragma("unroll") for (int i_ = 0; i_ < 16; ++i_) { X0[i_] -= dl_; X1[i_] -= dl_; negm[i_] -= dl_; } } \
      LW(4); GB(8,  NEXTK, { PK(3, 0, Y1, 8); PK(3, 1, Y1, 10); S1(Y1, 10); }); \
             GB(9,  NEXTK, { PK(3, 2, Y1, 12); PK(3, 3, Y1, 14); E1(X0, 0); }); \
      LW(4); GB(10, NEXTK, { S1(Y1, 11); E1(X0, 1); }); \
             GB(11, NEXTK, { E2(X0, 2); }); \
      LW(4); GB(12, NEXTK, { S1(Y1, 12); E2(X0, 4); }); \
             GB(13, NEXTK, { S1(Y1, 13); E2(X0, 6); }); \
      if (NEXTK) LW(2); else LW(0); \
             GB(14, NEXTK, { S1(Y1, 14); E2(X0, 8); }); \
             GB(15, NEXTK, { S1(Y1, 15); E2(X0, 10); E1(X0, 12); }); \
      if (__builtin_expect(resc_, 0)) { asm volatile("s_nop 7\n\ts_nop 7" : "+v"(o[0]), "+v"(o[1]), "+v"(o[2]), "+v"(o[3]) :: "memory"); \
        l *= alpha_; _Pragma("unroll") for (int d_ = 0; d_ < 4; ++d_) _Pragma("unroll") for (int i_ = 0; i_ < 16; ++i_) o[d_][i_] *= alpha_; } \
      sv = sv == 4 * SLOT + 16384 ? 16384 : sv + SLOT; } while (0)
    asm volatile("s_nop 1" : "+v"(negm), "+v"(sA0), "+v"(sA1) :: "memory");
#pragma unroll 1
    for (int t = 1; t < 15; t += 2) {
      PHASE_A(sB0, sB1, sA0, sA1); BAR(); sk = nxt5(sk); PHASE_B(sB0, sB1, sA1, true);
      PHASE_A(sA0, sA1, sB0, sB1); BAR(); sk = nxt5(sk); PHASE_B(sA0, sA1, sB1, true);
    }
    PHASE_A(sB0, sB1, sA0, sA1); BAR(); PHASE_B(sB0, sB1, sA1, false);
    {
      const LAS char* VP_ = L + sv;
#pragma unroll
      for (int i = 0; i < 16; ++i) VRD(i, VP_);
#pragma unroll
      for (int i = 13; i < 16; ++i) sB0[i] = EX(sB0[i]);
      const bf16x8 p0 = pack8(sB0, 0), p1 = pack8(sB0, 1);
#pragma unroll
      for (int db = 0; db < 4; ++db) o[db] = MFMA(vf[db], p0, o[db]);
#pragma unroll
      for (int i = 0; i < 16; ++i) sB1[i] = EX(sB1[i]);
#pragma unroll
      for (int db = 0; db < 4; ++db) o[db] = MFMA(vf[4 + db], p1, o[db]);
      const bf16x8 p2 = pack8(sB1, 0), p3 = pack8(sB1, 1);
#pragma unroll
      for (int db = 0; db < 4; ++db) o[db] = MFMA(vf[8 + db], p2, o[db]);
#pragma unroll
      for (int i = 0; i < 16; ++i) l += sB0[i];
#pragma unroll
      for (int db = 0; db < 4; ++db) o[db] = MFMA(vf[12 + db], p3, o[db]);
#pragma unroll
      for (int i = 0; i < 16; ++i) l += sB1[i];
      l = both_sum(l);
    }
    STAMP(7);
    const float inv = __builtin_amdgcn_rcpf(l);
    const __amdgpu_buffer_rsrc_t rs = SRD(ws + (unsigned)(((b * 4 + qb) * 256 + h * 8) * 1024), 8192);
#pragma unroll
    for (int db = 0; db < 4; ++db) {
#pragma unroll
      for (int i = 0; i < 16; ++i) o[db][i] *= inv;
#pragma unroll
      for (int s = 0; s < 2; ++s) __builtin_amdgcn_raw_buffer_store_b128(__builtin_bit_cast(u32x4, pack8(o[db], s)), rs, lane * 16 + (db * 2 + s) * 1024, 0, SC1);
    }
#pragma unroll
    for (int i = 0; i < 4; ++i) acc[i] = (f32x16){};
    asm volatile("s_waitcnt vmcnt(0)" : "+v"(acc[0]), "+v"(acc[1]), "+v"(acc[2]), "+v"(acc[3]) :: "memory");
    if (lane < 8) __hip_atomic_store(flags + (((b * 4 + (h >> 3)) * 8 + lane) * 32 + qb * 8 + (h & 7)), tag, RLX_AGENT);
  }
  STAMP(8);
  {
    BAR_L();
    STAMP(9);
    const __amdgpu_buffer_rsrc_t rso = SRD(ws + (size_t)(ob * 4 + qb) * 262144, 262144);
    u32x4 bq[2][8];
    bf16x8 wf[16];
    int sw = 4 * SLOT;
#define OL1(lb, lh, so_, i_) bq[lb][4 * (lh) + (i_)] = __builtin_amdgcn_raw_buffer_load_b128(rso, lane * 16 + (i_) * 1024, so_, SC1)
#define OLD4(buf, p, hf) do { const unsigned so_ = (unsigned)hmap(p) * 8192u + (hf) * 4096u; _Pragma("unroll") for (int i_ = 0; i_ < 4; ++i_) { OL1(buf, hf, so_, i_); SBAR(); } } while (0)
#define ORD(i, SP) wf[i] = rdtr((SP) + tbase + 4096 * ((i) >> 2) + 512 * ((i) & 3), (SP) + tbase + 4096 * ((i) >> 2) + 512 * ((i) & 3) + 2048)
#define GO(buf, hf, g, BODY) do { acc[(g) & 3] = MFMA(wf[g], __builtin_bit_cast(bf16x8, bq[buf][4 * (hf) + ((g) >> 2)]), acc[(g) & 3]); BODY; SBAR(); } while (0)
#define OHALF(buf, hf, S, N, SYNC, MORE, LD, lb, lh, lp) do { unsigned so_ = 0; SBAR(); \
      GO(buf, hf, 0, { ORD(6, S); so_ = (lp) < 8 ? (unsigned)hmap(lp) * 8192u + (lh) * 4096u : 0x7f000000u; asm volatile("" : "+s"(so_)); }); GO(buf, hf, 1, { ORD(7, S); if (LD) OL1(lb, lh, so_, 0); }); \
      GO(buf, hf, 2, { ORD(8, S); if (LD) OL1(lb, lh, so_, 1); }); GO(buf, hf, 3, { ORD(9, S); if (LD) OL1(lb, lh, so_, 2); }); GO(buf, hf, 4, { ORD(10, S); if (LD) OL1(lb, lh, so_, 3); }); \
      GO(buf, hf, 5, { ORD(11, S); ORD(12, S); }); GO(buf, hf, 6, { ORD(13, S); ORD(14, S); }); GO(buf, hf, 7, ORD(15, S)); GO(buf, hf, 8, {}); GO(buf, hf, 9, {}); GO(buf, hf, 10, {}); \
      if ((SYNC) == 1) { BAR_L(); SBAR(); } else if ((SYNC) == 2) { BAR(); SBAR(); } \
      GO(buf, hf, 11, if (MORE) ORD(0, N)); GO(buf, hf, 12, if (MORE) { ORD(1, N); ORD(2, N); }); GO(buf, hf, 13, if (MORE) ORD(3, N)); GO(buf, hf, 14, if (MORE) ORD(4, N)); GO(buf, hf, 15, if (MORE) ORD(5, N)); } while (0)
#define OSTAGE(buf, p, LAST, LD2) do { const LAS char* S_ = L + sw; sw = nxt5(sw); const LAS char* N_ = L + sw; \
      OHALF(buf, 0, S_, S_ + 16384, (LAST) ? 0 : 2, true, !(LAST), (buf) ^ 1, 1, (p) + 1); \
      OHALF(buf, 1, S_ + 16384, N_, (LAST) ? 0 : 1, !(LAST), LD2, buf, 0, (p) + 2); } while (0)
    OLD4(0, 0, 0); OLD4(0, 0, 1); OLD4(1, 1, 0);
    { const LAS char* S_ = L + sw; ORD(0, S_); SBAR(); ORD(1, S_); ORD(2, S_); SBAR(); ORD(3, S_); SBAR(); ORD(4, S_); SBAR(); ORD(5, S_); SBAR(); }
#pragma unroll 1
    for (int p = 0; p < 6; p += 2) { OSTAGE(0, p, false, true); OSTAGE(1, p + 1, false, true); }
    OSTAGE(0, 6, false, false); OSTAGE(1, 7, true, false);
    STAMP(10);
    const int tile = ob * 8 + nb;
    GAS u64* flags2 = (GAS u64*)(ws + WS_FLAGS2) + (tile * 16 + qb) * 16;
    {
      const __amdgpu_buffer_rsrc_t rp = SRD(ws + WS_PART + (size_t)tile * 131072 + (kq * 4 + qb) * 2048, 131072);
#pragma unroll
      for (int i = 1; i < 4; ++i)
#pragma unroll
        for (int g = 0; g < 2; ++g) __builtin_amdgcn_raw_buffer_store_b128(__builtin_bit_cast(u32x4, pack8(acc[i], g)), rp, lane * 16 + ((kq + i) & 3) * 32768u + g * 1024, 0, SC1);
      WAITV(0);
      if (lane < 4) __hip_atomic_store(flags2 + lane * 64 + kq, tag, RLX_AGENT);
    }
    STAMP(4);
#pragma unroll 1
    for (unsigned spins = 0; spins < SPIN_CAP; ++spins) {
      if (__all(__hip_atomic_load(flags2 + kq * 64 + (lane & 3), RLX_AGENT) == tag || (lane & 3) == kq)) break;
      __builtin_amdgcn_s_sleep(1);
    }
    __builtin_amdgcn_fence(__ATOMIC_ACQUIRE, "wavefront");
    STAMP(5);
    {
      const __amdgpu_buffer_rsrc_t rp = SRD(ws + WS_PART + (size_t)tile * 131072 + kq * 32768 + qb * 2048, 32768);
      u32x4 pv[3][2];
#pragma unroll
      for (int i = 1; i < 4; ++i)
#pragma unroll
        for (int g = 0; g < 2; ++g) pv[i - 1][g] = __builtin_amdgcn_raw_buffer_load_b128(rp, lane * 16 + g * 1024, ((kq + i) & 3) * 8192u, SC1);
      f32x16 sum = acc[0];
#pragma unroll
      for (int i = 0; i < 3; ++i)
#pragma unroll
        for (int e = 0; e < 8; ++e) { const unsigned w = pv[i][e >> 2][e & 3]; sum[2 * e] += __uint_as_float(w << 16); sum[2 * e + 1] += __uint_as_float(w & 0xffff0000u); }
      GAS char* orow = (GAS char*)out + (size_t)(ob * 128 + qb * 32 + r) * (DM * 2) + (nb * 128 + kq * 32) * 2 + hh * 16;
#pragma unroll
      for (int g = 0; g < 2; ++g) {
        unsigned a0 = cvtpk(sum[8 * g], sum[8 * g + 1]), a1 = cvtpk(sum[8 * g + 2], sum[8 * g + 3]);
        unsigned b0 = cvtpk(sum[8 * g + 4], sum[8 * g + 5]), b1 = cvtpk(sum[8 * g + 6], sum[8 * g + 7]);
        auto r0 = __builtin_amdgcn_permlane32_swap(a0, b0, false, false);
        auto r1 = __builtin_amdgcn_permlane32_swap(a1, b1, false, false);
        u32x4 w = {r0[0], r1[0], r0[1], r1[1]};
        *(GAS u32x4*)(orow + g * 32) = w;
      }
    }
    STAMP(11);
  }
}

extern "C" void kernel_launch(void* const* d_in, const int* in_sizes, int n_in, void* d_out, int out_size, void* d_ws, size_t ws_size, hipStream_t stream) {
  mha_fused<<<256, 512, 0, stream>>>((const char*)d_in[0], (const char*)d_in[1], (const char*)d_in[2], (const char*)d_in[3], (const char*)d_in[4], (char*)d_out, (char*)d_ws);
}
```

```cpp
#include <hip/hip_runtime.h>
#include <hip/hip_bf16.h>

#define KV_B 2
#define CODE_KB 23

typedef __bf16 bf16x8 __attribute__((ext_vector_type(8)));
typedef __bf16 bf16x2 __attribute__((ext_vector_type(2)));
typedef short s16x4 __attribute__((ext_vector_type(4)));
typedef short s16x8 __attribute__((ext_vector_type(8)));
typedef float f32x16 __attribute__((ext_vector_type(16)));
typedef float f32x4 __attribute__((ext_vector_type(4)));
typedef float f32x2 __attribute__((ext_vector_type(2)));
typedef unsigned u32x4 __attribute__((ext_vector_type(4)));
typedef unsigned long long u64;
#define LAS __attribute__((address_space(3)))
#define GAS __attribute__((address_space(1)))
#define DI __device__ __forceinline__
#define MFMA(a, b, c) __builtin_amdgcn_mfma_f32_32x32x16_bf16((a), (b), (c), 0, 0, 0)
#define SBAR() __builtin_amdgcn_sched_barrier(0)
#define RLX_AGENT __ATOMIC_RELAXED, __HIP_MEMORY_SCOPE_AGENT
#define SRD(p, n) __builtin_amdgcn_make_buffer_rsrc((void*)(p), 0, (n), 0x00020000)

constexpr int SQ = 128, DM = 1024, NH = 32, SK = 1024, DH = 128;
constexpr int SLOT = 32768, LDS_BYTES = 163840;
constexpr float C2 = 0.08838834764831845f * 1.4426950408889634f;
constexpr float THR = 8.0f;
constexpr unsigned WS_FLAGS = 8u << 20, WS_EPOCH = WS_FLAGS + 65536u, WS_FLAGS2 = 9u << 20, WS_PART = 16u << 20;
constexpr u64 EPOCH_INC = 0x9E3779B97F4A7C15ull;
constexpr unsigned SPIN_CAP = 1u << 22;
constexpr int SC1 = 16, NT = 2;

#define STAMP(i) do {} while (0)
#define LS() do {} while (0)

DI unsigned rfl(unsigned x) { return __builtin_amdgcn_readfirstlane(x); }
DI unsigned cvtpk(float lo, float hi) { f32x2 v = {lo, hi}; bf16x2 b = __builtin_convertvector(v, bf16x2); return __builtin_bit_cast(unsigned, b); }
DI bf16x8 pack8(const f32x16& x, int s) {
  u32x4 w = {cvtpk(x[8 * s + 0], x[8 * s + 1]), cvtpk(x[8 * s + 2], x[8 * s + 3]), cvtpk(x[8 * s + 4], x[8 * s + 5]), cvtpk(x[8 * s + 6], x[8 * s + 7])};
  return __builtin_bit_cast(bf16x8, w);
}
DI void dma16(__amdgpu_buffer_rsrc_t rs, unsigned voff, unsigned soff, unsigned ldsaddr) {
  asm volatile("s_mov_b32 m0, %0\n\ts_nop 0\n\tbuffer_load_dwordx4 %1, %2, %3 offen lds" ::"s"(ldsaddr), "v"(voff), "s"(rs), "s"(soff) : "memory");
}
#define DMA_PC(V, NTS) "buffer_load_dwordx4 " V ", %4, %0 offen " NTS "lds\n\t"
#define DMA_2(VA, VB, NTS) DMA_PC(VA, NTS) "s_add_u32 m0, m0, 0x400\n\ts_add_u32 %0, %0, %5\n\t" DMA_PC(VB, NTS) "s_add_u32 m0, m0, 0x400\n\ts_add_u32 %0, %0, %6\n\t"
template <int SEL> DI void dma_tile(__amdgpu_buffer_rsrc_t rs, unsigned voe, unsigned voo, unsigned soff, unsigned p1, unsigned p2, unsigned ldsaddr) {
  const unsigned d2 = p2 - p1;
  if constexpr (SEL == 0)
    asm volatile("s_mov_b32 m0, %1\n\ts_nop 0\n\t" DMA_2("%2", "%3", "") DMA_2("%2", "%3", "") DMA_2("%2", "%3", "") DMA_2("%2", "%3", "") DMA_2("%2", "%3", "") DMA_2("%2", "%3", "") DMA_2("%2", "%3", "") DMA_2("%2", "%3", "")
                 : "+&s"(soff) : "s"(ldsaddr), "v"(voe), "v"(voo), "s"(rs), "s"(p1), "s"(d2) : "memory", "scc");
  else
    asm volatile("s_mov_b32 m0, %1\n\ts_nop 0\n\t" DMA_2("%2", "%2", "nt ") DMA_2("%3", "%3", "nt ") DMA_2("%2", "%2", "nt ") DMA_2("%3", "%3", "nt ") DMA_2("%2", "%2", "nt ") DMA_2("%3", "%3", "nt ") DMA_2("%2", "%2", "nt ") DMA_2("%3", "%3", "nt ")
                 : "+&s"(soff) : "s"(ldsaddr), "v"(voe), "v"(voo), "s"(rs), "s"(p1), "s"(d2) : "memory", "scc");
}
DI bf16x8 rd128(const LAS char* p) { return *(const LAS bf16x8*)p; }
DI bf16x8 rdtr(const LAS char* lo, const LAS char* up) {
  s16x4 a = __builtin_amdgcn_ds_read_tr16_b64_v4i16((LAS s16x4*)lo);
  s16x4 b = __builtin_amdgcn_ds_read_tr16_b64_v4i16((LAS s16x4*)up);
  s16x8 c = __builtin_shufflevector(a, b, 0, 1, 2, 3, 4, 5, 6, 7);
  return __builtin_bit_cast(bf16x8, c);
}
#define BAR() asm volatile("s_barrier" ::: "memory")
#define BAR_L() asm volatile("s_waitcnt lgkmcnt(0)\n\ts_barrier" ::: "memory")
#define WAITV(N) asm volatile("s_waitcnt vmcnt(" #N ")" ::: "memory")
#define WAITV_BAR(N) asm volatile("s_waitcnt vmcnt(" #N ")\n\ts_barrier" ::: "memory")
#define MX3(a, b, c) __builtin_fmaxf(__builtin_fmaxf((a), (b)), (c))
DI float both_max(float x) { auto r = __builtin_amdgcn_permlane32_swap(__float_as_uint(x), __float_as_uint(x), false, false); return __builtin_fmaxf(__uint_as_float(r[0]), __uint_as_float(r[1])); }
DI float both_sum(float x) { auto r = __builtin_amdgcn_permlane32_swap(__float_as_uint(x), __float_as_uint(x), false, false); return __uint_as_float(r[0]) + __uint_as_float(r[1]); }
DI void mfma_acc(f32x16& d, const bf16x8& a, const bf16x8& b) { asm volatile("v_mfma_f32_32x32x16_bf16 %0, %1, %2, %0" : "+v"(d) : "v"(a), "v"(b) : "memory"); }
DI void mfma_new(f32x16& d, const bf16x8& a, const bf16x8& b, const f32x16& c) { asm volatile("v_mfma_f32_32x32x16_bf16 %0, %1, %2, %3" : "=&v"(d) : "v"(a), "v"(b), "v"(c) : "memory"); }
DI float aexp(float x) { asm volatile("v_exp_f32 %0, %0" : "+v"(x) :: "memory"); return x; }
DI unsigned acvt(float a, float b) { unsigned r; asm volatile("v_cvt_pk_bf16_f32 %0, %1, %2" : "=v"(r) : "v"(a), "v"(b) : "memory"); return r; }
DI void aadd1(float& l, float a) { asm volatile("v_add_f32 %0, %0, %1" : "+v"(l) : "v"(a) : "memory"); }
DI void aadd2(float& l, float a, float b) { asm volatile("v_add_f32 %0, %0, %1\n\tv_add_f32 %0, %0, %2" : "+&v"(l) : "v"(a), "v"(b) : "memory"); }
DI void aadd3(float& l, float a, float b, float c) { asm volatile("v_add_f32 %0, %0, %1\n\tv_add_f32 %0, %0, %2\n\tv_add_f32 %0, %0, %3" : "+&v"(l) : "v"(a), "v"(b), "v"(c) : "memory"); }
DI void aadd4(float& l, float a, float b, float c, float d) { asm volatile("v_add_f32 %0, %0, %1\n\tv_add_f32 %0, %0, %2\n\tv_add_f32 %0, %0, %3\n\tv_add_f32 %0, %0, %4" : "+&v"(l) : "v"(a), "v"(b), "v"(c), "v"(d) : "memory"); }
DI float amax7(float a, float b, float c, float d, float e, float f, float g) { float r; asm volatile("v_max3_f32 %0, %1, %2, %3\n\tv_max3_f32 %0, %0, %4, %5\n\tv_max3_f32 %0, %0, %6, %7" : "=&v"(r) : "v"(a), "v"(b), "v"(c), "v"(d), "v"(e), "v"(f), "v"(g) : "memory"); return r; }
DI void amax6(float& r, float a, float b, float c, float d, float e, float f) { asm volatile("v_max3_f32 %0, %0, %1, %2\n\tv_max3_f32 %0, %0, %3, %4\n\tv_max3_f32 %0, %0, %5, %6" : "+&v"(r) : "v"(a), "v"(b), "v"(c), "v"(d), "v"(e), "v"(f) : "memory"); }
DI void amax8(float& r, float a, float b, float c, float d, float e, float f, float g, float h) { asm volatile("v_max3_f32 %0, %0, %1, %2\n\tv_max3_f32 %0, %0, %3, %4\n\tv_max3_f32 %0, %0, %5, %6\n\tv_max3_f32 %0, %0, %7, %8" : "+&v"(r) : "v"(a), "v"(b), "v"(c), "v"(d), "v"(e), "v"(f), "v"(g), "v"(h) : "memory"); }
DI void amax3z(float& r, float a, float b, float c) { asm volatile("v_max3_f32 %0, %0, %1, %2\n\tv_max3_f32 %0, %0, %3, 0" : "+&v"(r) : "v"(a), "v"(b), "v"(c) : "memory"); }
DI int nxt5(int s) { return s == 4 * SLOT ? 0 : s + SLOT; }
struct Tile { u32x4 v[16]; };
DI void tile_init(Tile& t) {
#pragma unroll
  for (int p = 0; p < 16; ++p) asm volatile("" : "=v"(t.v[p]));
}
template <int AUX, int IMM> DI void ld16(u32x4& d, __amdgpu_buffer_rsrc_t rs, unsigned voff, unsigned soff) {
  if constexpr (AUX == NT) asm volatile("buffer_load_dwordx4 %0, %1, %2, %3 offen offset:%4 nt" : "+v"(d) : "v"(voff), "s"(rs), "s"(soff), "i"(IMM) : "memory");
  else                     asm volatile("buffer_load_dwordx4 %0, %1, %2, %3 offen offset:%4" : "+v"(d) : "v"(voff), "s"(rs), "s"(soff), "i"(IMM) : "memory");
}
template <int P> DI void wr16(const u32x4& d, unsigned addr) { asm volatile("ds_write_b128 %0, %1 offset:%2" ::"v"(addr), "v"(d), "i"(P * 1024) : "memory"); }
DI void tile_wr(const Tile& t, unsigned addr) {
  wr16<0>(t.v[0], addr); wr16<1>(t.v[1], addr); wr16<2>(t.v[2], addr); wr16<3>(t.v[3], addr); wr16<4>(t.v[4], addr); wr16<5>(t.v[5], addr); wr16<6>(t.v[6], addr); wr16<7>(t.v[7], addr);
  wr16<8>(t.v[8], addr); wr16<9>(t.v[9], addr); wr16<10>(t.v[10], addr); wr16<11>(t.v[11], addr); wr16<12>(t.v[12], addr); wr16<13>(t.v[13], addr); wr16<14>(t.v[14], addr); wr16<15>(t.v[15], addr);
}

__global__ __launch_bounds__(512, 2) void mha_fused(const char* x, const char* kc, const char* vc, const char* wq, const char* wo, char* out, char* ws) {
  __shared__ __attribute__((aligned(16))) char lds[LDS_BYTES];
  u64 pc0; asm volatile("s_getpc_b64 %0" : "=s"(pc0), "+s"(x), "+s"(kc), "+s"(vc), "+s"(wq), "+s"(wo), "+s"(out), "+s"(ws));
  const int tid = threadIdx.x, lane = tid & 63;
  const int wid = rfl(tid >> 6);
  const int wg = blockIdx.x;
  const int xcd = wg & 7, j = wg >> 3;
  const int b = j & 7, h = xcd * 4 + (j >> 3);
  const bool late = (j ^ (j >> 3)) & 1;
  const int kq = xcd >> 1, ob = (xcd & 1) * 4 + (j >> 3), nb = j & 7;
  auto hmap = [&](int p) { return 8 * kq + 2 * (p & 3) + ((ob & 1) ^ (p >> 2)); };
  const unsigned lds0 = (unsigned)(size_t)(LAS char*)lds;
  GAS u64* flags = (GAS u64*)(ws + WS_FLAGS);

  if (__builtin_expect(wid >= 4, 1)) {
    const unsigned r8 = (lane >> 2) & 7, sub = lane >> 5, c4 = lane & 3;
    const unsigned hb = wid & 1;
    const unsigned xe = hb ? c4 : (c4 ^ (r8 >> 2)), xo = hb ? c4 : (c4 ^ (2 + (r8 >> 2)));
    const unsigned ldh = lds0 + hb * 16384u;
    const __amdgpu_buffer_rsrc_t rcode = SRD(pc0 & ~255ull, CODE_KB * 1024);
    if (__builtin_expect(wid >= 6, 0)) {
      const __amdgpu_buffer_rsrc_t rs = SRD((hb ? vc : kc) + (size_t)(b * NH + h) * SK * DH * 2, SK * DH * 2);
      const unsigned voe = r8 * 256u + sub * 64u + 16u * xe, voo = r8 * 256u + sub * 64u + 16u * xo;
      const int krot = (j + 5 * xcd) & 15;
      auto tile_dma = [&](int t, int so) { dma_tile<1>(rs, voe, voo, ((t + krot) & 15) * 16384u, 128u, 2048u, ldh + so); };
      const __amdgpu_buffer_rsrc_t rwo = SRD(wo + nb * 256, NH * DH * DM * 2 - nb * 256);
      const unsigned vw0 = (hb * 64u + r8) * 2048u + 16u * c4 + 64u * ((kq + sub) & 3), vw1 = (hb * 64u + r8) * 2048u + 16u * c4 + 64u * ((kq + 2 + sub) & 3);
      auto w_dma = [&](int p, int so) { dma_tile<0>(rwo, vw0, vw1, (unsigned)hmap(p) * (128u * 2048u), 0u, 16384u, ldh + so); };
      dma16(rcode, lane * 16u, ((j & 15) * 2 + hb) * 1024u, ldh + 4 * SLOT);
      int sl = 3 * SLOT;
      int n0 = 5;
      if (late) {
        const unsigned wdst = ldh + lane * 16u;
        Tile R0, R1, R2;
        tile_init(R0); tile_init(R1); tile_init(R2);
        auto tile_ld = [&](Tile& R, int t) {
#pragma unroll
          for (int q = 0; q < 4; ++q) {
            const unsigned so = ((t + krot) & 15) * 16384u + q * 4096u;
            ld16<NT, 0>(R.v[4 * q], rs, voe, so); ld16<NT, 128>(R.v[4 * q + 1], rs, voe, so);
            ld16<NT, 2048>(R.v[4 * q + 2], rs, voo, so); ld16<NT, 2048 + 128>(R.v[4 * q + 3], rs, voo, so);
          }
        };
#pragma unroll 1
        for (int c = -1; c <= KV_B; ++c) BAR();
        tile_dma(0, 3 * SLOT); tile_dma(1, 4 * SLOT); tile_ld(R0, 2);
        WAITV(32); tile_ld(R1, 3);
        WAITV(32); tile_ld(R2, 4);
#pragma unroll 1
        for (int c = KV_B + 1; c <= 14; ++c) BAR();
        WAITV_BAR(0);
        tile_wr(R0, wdst);
        BAR_L(); tile_wr(R1, wdst + SLOT);
        if (hb) BAR_L();
        tile_wr(R2, wdst + 2 * SLOT);
        asm volatile("s_waitcnt lgkmcnt(0)" ::: "memory");
      } else {
#pragma unroll 1
        for (int c = -1; c <= 12 + (int)hb; ++c) BAR();
        __builtin_amdgcn_s_sleep(6);
        tile_dma(0, sl); sl = nxt5(sl);
        n0 = 1;
      }
#pragma unroll 1
      for (int n = n0; n <= 15; ++n) { WAITV_BAR(32); tile_dma(n, sl); sl = nxt5(sl); }
      if (!hb) {
        WAITV_BAR(32); WAITV_BAR(16); w_dma(0, sl); sl = nxt5(sl);
        WAITV_BAR(16); w_dma(1, sl); sl = nxt5(sl);
        BAR(); w_dma(2, sl); sl = nxt5(sl);
      } else {
#pragma unroll 1
        for (int s = 0; s <= 2; ++s) { WAITV_BAR(32); w_dma(s, sl); sl = nxt5(sl); }
      }
      WAITV_BAR(32);
      WAITV(31);
      __builtin_amdgcn_s_sleep(8);
      w_dma(3, sl); sl = nxt5(sl); w_dma(4, sl);
#pragma unroll 1
      for (int p = 5; p < 8; ++p) { BAR(); WAITV_BAR(47); sl = nxt5(sl); w_dma(p, sl); }
      BAR(); WAITV_BAR(47); BAR(); WAITV_BAR(32); BAR(); WAITV_BAR(16); BAR(); WAITV_BAR(0);
      return;
    }
    u64 tag = 0;
    {
      const int qrot = (2 * (j & 7) + 4 * (j >> 3)) & 15;
      u64 qbase = hb ? (u64)wq : (u64)x; unsigned qoff = hb ? h * DH * 2 : b * SQ * DM * 2, qn = hb ? DM * NH * DH * 2 : (b + 1) * SQ * DM * 2;
      unsigned rsh = hb ? 13u : 11u, cstr = hb ? 64u * 8192u : 128u, p1 = hb ? 128u : 16384u, p2 = hb ? 65536u : 32768u;
      asm volatile("" : "+s"(qbase), "+s"(qoff), "+s"(qn), "+s"(rsh), "+s"(cstr), "+s"(p1), "+s"(p2));
      const __amdgpu_buffer_rsrc_t rs = SRD(qbase, qn);
      const unsigned voe = (r8 << rsh) + sub * 64u + 16u * xe, voo = (r8 << rsh) + sub * 64u + 16u * xo;
      int sl = 2 * SLOT;
      tag = __hip_atomic_load((GAS u64*)(ws + WS_EPOCH), RLX_AGENT);
#pragma unroll 1
      for (int c = 0; c < 16; ++c) {
        dma_tile<0>(rs, voe, voo, qoff + ((c + qrot) & 15) * cstr, p1, p2, ldh + sl); sl = sl == 2 * SLOT ? 0 : sl + SLOT;
        if (c == 2) { WAITV_BAR(32); LS(); }
        if (c >= 2) { WAITV_BAR(16); LS(); }
      }
      WAITV_BAR(0); LS(); BAR(); LS();
    }
    tag += EPOCH_INC;
    {
      u64 seen = 0;
      const GAS u64* myflag = flags + (((ob * 4 + kq) * 8 + nb) * 32 + (lane & 31));
      auto gate = [&](u64 need) {
        if (hb) return;
#pragma unroll 1
        for (unsigned spins = 0; (seen & need) != need && spins < SPIN_CAP; ++spins) {
          seen = __ballot(__hip_atomic_load(myflag, RLX_AGENT) == tag);
          if ((seen & need) != need) __builtin_amdgcn_s_sleep(1);
        }
      };
      auto hbit = [&](int p) { return 0x01010101ull << (hmap(p) & 7); };
#pragma unroll 1
      for (int t = 0; t <= 10; ++t) { BAR(); LS(); }
      u32x4 d0; asm volatile("" : "=v"(d0));
      ld16<0, 0>(d0, rcode, lane * 16u, ((j >> 1) * 2 + hb) * 1024u);
#pragma unroll 1
      for (int t = 11; t <= 14; ++t) { BAR(); LS(); }
      WAITV_BAR(0); LS();
      asm volatile("" ::"v"(d0));
      gate(hbit(0) | hbit(1)); BAR(); LS();
#pragma unroll 1
      for (int m = 0; m < 7; ++m) { if (m < 6) gate(hbit(m + 2)); BAR(); BAR(); LS(); }
    }
    if (wg == 0 && wid == 4) {
#pragma unroll 1
      for (unsigned spins = 0; spins < SPIN_CAP; ++spins) {
        bool ok = true;
#pragma unroll
        for (int i = 0; i < 16; ++i) ok &= __hip_atomic_load(flags + ((2 * i + (lane >> 5)) * 256 + (lane & 31)), RLX_AGENT) == tag;
        if (__all(ok)) break;
        __builtin_amdgcn_s_sleep(2);
      }
      if (lane == 0) __hip_atomic_store((GAS u64*)(ws + WS_EPOCH), tag, RLX_AGENT);
    }
    return;
  }
  STAMP(0);
  const u64 tag = __hip_atomic_load((GAS u64*)(ws + WS_EPOCH), RLX_AGENT) + EPOCH_INC;
  const int r = lane & 31, hh = lane >> 5, qb = wid;
  const int tq = (lane >> 2) & 3, tp = lane & 3, blk = (lane >> 4) & 1;
  const LAS char* L = (const LAS char*)lds;
  const int rowe = 64 * (r & 7) + 16 * (hh ^ ((r >> 2) & 3));
  const int kbase_e = 2048 * (r >> 3) + rowe, kbase_o = kbase_e ^ 32;
  const int tbase = hh * 256 + tq * 64 + blk * 32 + tp * 8;
  bf16x8 qf[8];
  bf16x8 kf[16], vf[16];
  f32x16 acc[4] = {};
#define KRD(i, SP) kf[i] = rd128((SP) + ((((i) >> 1) & 1) ? kbase_o : kbase_e) + 512 * ((i) >> 2) + 8192 * ((i) & 1))
  {
    const int xbase_e = 1024 * (4 * qb + (r >> 3)) + rowe, xbase_o = xbase_e ^ 32;
    const int pp = (tp == 1) ? 2 : (tp == 2) ? 1 : tp;
    const int wbase = 16384 + hh * 2048 + tq * 64 + blk * 32 + pp * 8;
    bf16x8 xf[4], wf[16];
    int sl = 2 * SLOT;
#define WRD(i, SP) wf[i] = rdtr((SP) + wbase + 4096 * ((i) >> 2) + 512 * ((i) & 3), (SP) + wbase + 4096 * ((i) >> 2) + 512 * ((i) & 3) + 256)
#define XRD(ks, SP) xf[ks] = rd128((SP) + (((ks) & 1) ? xbase_o : xbase_e) + 512 * ((ks) >> 1))
#define GQ(g, BODY) do { acc[(g) & 3] = MFMA(wf[g], xf[(g) >> 2], acc[(g) & 3]); BODY; SBAR(); } while (0)
    __builtin_amdgcn_s_waitcnt(0xC07F);
    BAR();
    STAMP(1);
    { const LAS char* S = L + sl; XRD(0, S); WRD(0, S); SBAR(); WRD(1, S); WRD(2, S); SBAR(); WRD(3, S); XRD(1, S); SBAR(); WRD(4, S); SBAR(); WRD(5, S); SBAR(); }
#define QCHUNK(LAST) do { const LAS char* S = L + sl; sl = sl == 2 * SLOT ? 0 : sl + SLOT; const LAS char* N = (LAST) ? L + 3 * SLOT : L + sl; SBAR(); \
      GQ(0, { WRD(6, S); XRD(2, S); }); GQ(1, { WRD(7, S); XRD(3, S); }); GQ(2, { WRD(8, S); }); GQ(3, { WRD(9, S); WRD(10, S); }); GQ(4, WRD(11, S)); GQ(5, { WRD(12, S); WRD(13, S); }); \
      GQ(6, { WRD(14, S); }); GQ(7, WRD(15, S)); GQ(8, {}); GQ(9, {}); GQ(10, {}); \
      BAR_L(); SBAR(); GQ(11, {}); \
      if (!(LAST)) { XRD(0, N); WRD(0, N); SBAR(); GQ(12, { WRD(1, N); WRD(2, N); }); GQ(13, { WRD(3, N); XRD(1, N); }); GQ(14, { WRD(4, N); }); GQ(15, WRD(5, N)); } \
      else { GQ(12, { KRD(0, N); KRD(1, N); }); GQ(13, { KRD(2, N); KRD(3, N); }); GQ(14, { KRD(4, N); KRD(5, N); }); GQ(15, { KRD(6, N); KRD(7, N); }); } } while (0)
#pragma unroll 1
    for (int c = 0; c < 15; ++c) QCHUNK(false);
    QCHUNK(true);
#pragma unroll
    for (int d = 0; d < 4; ++d)
#pragma unroll
      for (int i = 0; i < 16; ++i) acc[d][i] *= C2;
#pragma unroll
    for (int s = 0; s < 8; ++s) qf[s] = pack8(acc[s >> 1], s & 1);
  }
  STAMP(2);
  {
    f32x16 o[4] = {};
    float l = 0.f;
    f32x16 sA0, sA1, sB0, sB1, negm;
    u32x4 pw[4];
    const f32x16 zero16 = {};
    int sk = 3 * SLOT;
    int sv = 3 * SLOT + 16384;
#define VRD(i, SP) vf[i] = rdtr((SP) + tbase + 4096 * ((i) >> 2) + 512 * ((i) & 3), (SP) + tbase + 4096 * ((i) >> 2) + 512 * ((i) & 3) + 2048)
#define EX(v) __builtin_amdgcn_exp2f(v)
#define PF(k) __builtin_bit_cast(bf16x8, pw[k])
#define LW(n) __builtin_amdgcn_s_waitcnt(0xC07F | ((n) << 8))
    {
      const LAS char* KP = L + sk;
#pragma unroll
      for (int i = 8; i < 16; ++i) KRD(i, KP);
      sA0 = MFMA(kf[0], qf[0], zero16); sA1 = MFMA(kf[1], qf[0], zero16);
#pragma unroll
      for (int s = 1; s < 8; ++s) { sA0 = MFMA(kf[2 * s], qf[s], sA0); sA1 = MFMA(kf[2 * s + 1], qf[s], sA1); }
      BAR_L();
      STAMP(3);
      sk = nxt5(sk);
      const LAS char* KN = L + sk;
      KRD(0, KN); SBAR(); KRD(1, KN); SBAR(); KRD(2, KN); SBAR(); KRD(3, KN); SBAR();
      float a = MX3(sA0[0], sA0[1], sA1[0]); a = MX3(a, sA1[1], sA0[2]);
#pragma unroll
      for (int i = 3; i < 16; i += 2) a = MX3(a, sA0[i], (i + 1 < 16) ? sA0[i + 1] : sA0[i]);
#pragma unroll
      for (int i = 2; i < 16; i += 2) a = MX3(a, sA1[i], sA1[i + 1]);
      const float m = both_max(a);
#pragma unroll
      for (int i = 0; i < 16; ++i) { sA0[i] -= m; sA1[i] -= m; negm[i] = -m; }
#pragma unroll
      for (int i = 0; i < 13; ++i) sA0[i] = EX(sA0[i]);
    }
#define GA(i, X, BODY) do { if ((i) < 2) mfma_new(X, kf[i], qf[0], negm); else mfma_acc(X, kf[i], qf[(i) >> 1]); if ((i) + 4 < 16) { KRD(((i) + 4) & 15, KP_); } else { VRD((i) & 3, VP_); } BODY; } while (0)
#define E1(Y, e) do { Y[e] = aexp(Y[e]); } while (0)
#define E2(Y, e) do { Y[e] = aexp(Y[e]); Y[e + 1] = aexp(Y[e + 1]); } while (0)
#define S1(Y, e) aadd1(l, Y[e])
#define S2(Y, e) aadd2(l, Y[e], Y[e + 1])
#define S3(Y, e) aadd3(l, Y[e], Y[e + 1], Y[e + 2])
#define S4(Y, e) aadd4(l, Y[e], Y[e + 1], Y[e + 2], Y[e + 3])
#define PK(k, d, Y, e) do { pw[k][d] = acvt(Y[e], Y[e + 1]); } while (0)
#define PHASE_A(X0, X1, Y0, Y1) do { const LAS char* KP_ = L + sk; const LAS char* VP_ = L + sv; \
      LW(2); GA(0,  X0, { E2(Y0, 13); }); \
             GA(1,  X1, { PK(0, 0, Y0, 0); E1(Y0, 15); E1(Y1, 0); }); \
      LW(2); GA(2,  X0, { E2(Y1, 1); }); \
             GA(3,  X1, { PK(0, 1, Y0, 2); E2(Y1, 3); }); \
      LW(2); GA(4,  X0, { E2(Y1, 5); asm volatile("" ::"v"(negm));   }); \
             GA(5,  X1, { PK(0, 2, Y0, 4); E2(Y1, 7); }); \
      LW(2); GA(6,  X0, { E2(Y1, 9); }); \
             GA(7,  X1, { PK(0, 3, Y0, 6); E2(Y1, 11); }); \
      LW(2); GA(8,  X0, { E2(Y1, 13); }); \
             GA(9,  X1, { PK(1, 0, Y0, 8); S2(Y0, 0); E1(Y1, 15); }); \
      LW(2); GA(10, X0, { PK(1, 1, Y0, 10); S3(Y0, 2); }); \
             GA(11, X1, { PK(1, 2, Y0, 12); S4(Y0, 5); }); \
      LW(2); GA(12, X0, { PK(1, 3, Y0, 14); S2(Y0, 9); }); \
             GA(13, X1, { S4(Y0, 11); }); \
      LW(4); GA(14, X0, { aadd3(l, Y0[15], Y1[0], Y1[1]); }); \
             GA(15, X1, { S2(Y1, 2); }); \
      } while (0)
#define GB(i, NEXTK, BODY) do { mfma_acc(o[(i) & 3], vf[i], PF((i) >> 2)); if ((i) + 4 < 16) { VRD(((i) + 4) & 15, VP_); } else if (NEXTK) { KRD((i) & 3, KN_); } BODY; } while (0)
#define PHASE_B(X0, X1, Y1, NEXTK) do { const LAS char* VP_ = L + sv; const LAS char* KN_ = L + sk; float a_; float alpha_ = 1.f; bool resc_ = false; \
      LW(4); GB(0, NEXTK, { PK(2, 0, Y1, 0); S2(Y1, 4); }); \
             GB(1, NEXTK, { PK(2, 1, Y1, 2); PK(2, 2, Y1, 4); PK(2, 3, Y1, 6); S1(Y1, 6); }); \
      LW(4); GB(2, NEXTK, { a_ = amax7(X0[0], X0[1], X0[2], X0[3], X0[4], X0[5], X0[6]); }); \
             GB(3, NEXTK, { amax8(a_, X0[7], X0[8], X0[9], X0[10], X0[11], X0[12], X0[13], X0[14]); }); \
      LW(4); GB(4, NEXTK, { amax6(a_, X0[15], X1[0], X1[1], X1[2], X1[3], X1[4]); }); \
             GB(5, NEXTK, { amax8(a_, X1[5], X1[6], X1[7], X1[8], X1[9], X1[10], X1[11], X1[12]); }); \
      LW(4); GB(6, NEXTK, { amax3z(a_, X1[13], X1[14], X1[15]); S1(Y1, 7); }); \
             GB(7, NEXTK, { S2(Y1, 8); }); \
      if (__builtin_expect(__any(a_ > THR), 0)) { const float dl_ = both_max(a_); alpha_ = __builtin_amdgcn_exp2f(-dl_); resc_ = true; \
        _Pragma("unroll") for (int i_ = 0; i_ < 16; ++i_) { X0[i_] -= dl_; X1[i_] -= dl_; negm[i_] -= dl_; } } \
      LW(4); GB(8,  NEXTK, { PK(3, 0, Y1, 8); PK(3, 1, Y1, 10); S1(Y1, 10); }); \
             GB(9,  NEXTK, { PK(3, 2, Y1, 12); PK(3, 3, Y1, 14); E1(X0, 0); }); \
      LW(4); GB(10, NEXTK, { S1(Y1, 11); E1(X0, 1); }); \
             GB(11, NEXTK, { E2(X0, 2); }); \
      LW(4); GB(12, NEXTK, { S1(Y1, 12); E2(X0, 4); }); \
             GB(13, NEXTK, { S1(Y1, 13); E2(X0, 6); }); \
      if (NEXTK) LW(2); else LW(0); \
             GB(14, NEXTK, { S1(Y1, 14); E2(X0, 8); }); \
             GB(15, NEXTK, { S1(Y1, 15); E2(X0, 10); E1(X0, 12); }); \
      if (__builtin_expect(resc_, 0)) { asm volatile("s_nop 7\n\ts_nop 7" : "+v"(o[0]), "+v"(o[1]), "+v"(o[2]), "+v"(o[3]) :: "memory"); \
        l *= alpha_; _Pragma("unroll") for (int d_ = 0; d_ < 4; ++d_) _Pragma("unroll") for (int i_ = 0; i_ < 16; ++i_) o[d_][i_] *= alpha_; } \
      sv = sv == 4 * SLOT + 16384 ? 16384 : sv + SLOT; } while (0)
    asm volatile("s_nop 1" : "+v"(negm), "+v"(sA0), "+v"(sA1) :: "memory");
#pragma unroll 1
    for (int t = 1; t < 15; t += 2) {
      PHASE_A(sB0, sB1, sA0, sA1); BAR(); sk = nxt5(sk); PHASE_B(sB0, sB1, sA1, true);
      PHASE_A(sA0, sA1, sB0, sB1); BAR(); sk = nxt5(sk); PHASE_B(sA0, sA1, sB1, true);
    }
    PHASE_A(sB0, sB1, sA0, sA1); BAR(); PHASE_B(sB0, sB1, sA1, false);
    {
      const LAS char* VP_ = L + sv;
#pragma unroll
      for (int i = 0; i < 16; ++i) VRD(i, VP_);
#pragma unroll
      for (int i = 13; i < 16; ++i) sB0[i] = EX(sB0[i]);
      const bf16x8 p0 = pack8(sB0, 0), p1 = pack8(sB0, 1);
#pragma unroll
      for (int db = 0; db < 4; ++db) o[db] = MFMA(vf[db], p0, o[db]);
#pragma unroll
      for (int i = 0; i < 16; ++i) sB1[i] = EX(sB1[i]);
#pragma unroll
      for (int db = 0; db < 4; ++db) o[db] = MFMA(vf[4 + db], p1, o[db]);
      const bf16x8 p2 = pack8(sB1, 0), p3 = pack8(sB1, 1);
#pragma unroll
      for (int db = 0; db < 4; ++db) o[db] = MFMA(vf[8 + db], p2, o[db]);
#pragma unroll
      for (int i = 0; i < 16; ++i) l += sB0[i];
#pragma unroll
      for (int db = 0; db < 4; ++db) o[db] = MFMA(vf[12 + db], p3, o[db]);
#pragma unroll
      for (int i = 0; i < 16; ++i) l += sB1[i];
      l = both_sum(l);
    }
    STAMP(7);
    const float inv = __builtin_amdgcn_rcpf(l);
    const __amdgpu_buffer_rsrc_t rs = SRD(ws + (unsigned)(((b * 4 + qb) * 256 + h * 8) * 1024), 8192);
#pragma unroll
    for (int db = 0; db < 4; ++db) {
#pragma unroll
      for (int i = 0; i < 16; ++i) o[db][i] *= inv;
#pragma unroll
      for (int s = 0; s < 2; ++s) __builtin_amdgcn_raw_buffer_store_b128(__builtin_bit_cast(u32x4, pack8(o[db], s)), rs, lane * 16 + (db * 2 + s) * 1024, 0, SC1);
    }
#pragma unroll
    for (int i = 0; i < 4; ++i) acc[i] = (f32x16){};
    asm volatile("s_waitcnt vmcnt(0)" : "+v"(acc[0]), "+v"(acc[1]), "+v"(acc[2]), "+v"(acc[3]) :: "memory");
    if (lane < 8) __hip_atomic_store(flags + (((b * 4 + (h >> 3)) * 8 + lane) * 32 + qb * 8 + (h & 7)), tag, RLX_AGENT);
  }
  STAMP(8);
  {
    BAR_L();
    STAMP(9);
    const __amdgpu_buffer_rsrc_t rso = SRD(ws + (size_t)(ob * 4 + qb) * 262144, 262144);
    u32x4 bq[2][8];
    bf16x8 wf[16];
    int sw = 4 * SLOT;
#define OL1(lb, lh, so_, i_) bq[lb][4 * (lh) + (i_)] = __builtin_amdgcn_raw_buffer_load_b128(rso, lane * 16 + (i_) * 1024, so_, SC1)
#define OLD4(buf, p, hf) do { const unsigned so_ = (unsigned)hmap(p) * 8192u + (hf) * 4096u; _Pragma("unroll") for (int i_ = 0; i_ < 4; ++i_) { OL1(buf, hf, so_, i_); SBAR(); } } while (0)
#define ORD(i, SP) wf[i] = rdtr((SP) + tbase + 4096 * ((i) >> 2) + 512 * ((i) & 3), (SP) + tbase + 4096 * ((i) >> 2) + 512 * ((i) & 3) + 2048)
#define GO(buf, hf, g, BODY) do { acc[(g) & 3] = MFMA(wf[g], __builtin_bit_cast(bf16x8, bq[buf][4 * (hf) + ((g) >> 2)]), acc[(g) & 3]); BODY; SBAR(); } while (0)
#define OHALF(buf, hf, S, N, SYNC, MORE, LD, lb, lh, lp) do { unsigned so_ = 0; SBAR(); \
      GO(buf, hf, 0, { ORD(6, S); so_ = (lp) < 8 ? (unsigned)hmap(lp) * 8192u + (lh) * 4096u : 0x7f000000u; asm volatile("" : "+s"(so_)); }); GO(buf, hf, 1, { ORD(7, S); if (LD) OL1(lb, lh, so_, 0); }); \
      GO(buf, hf, 2, { ORD(8, S); if (LD) OL1(lb, lh, so_, 1); }); GO(buf, hf, 3, { ORD(9, S); if (LD) OL1(lb, lh, so_, 2); }); GO(buf, hf, 4, { ORD(10, S); if (LD) OL1(lb, lh, so_, 3); }); \
      GO(buf, hf, 5, { ORD(11, S); ORD(12, S); }); GO(buf, hf, 6, { ORD(13, S); ORD(14, S); }); GO(buf, hf, 7, ORD(15, S)); GO(buf, hf, 8, {}); GO(buf, hf, 9, {}); GO(buf, hf, 10, {}); \
      if ((SYNC) == 1) { BAR_L(); SBAR(); } else if ((SYNC) == 2) { BAR(); SBAR(); } \
      GO(buf, hf, 11, if (MORE) ORD(0, N)); GO(buf, hf, 12, if (MORE) { ORD(1, N); ORD(2, N); }); GO(buf, hf, 13, if (MORE) ORD(3, N)); GO(buf, hf, 14, if (MORE) ORD(4, N)); GO(buf, hf, 15, if (MORE) ORD(5, N)); } while (0)
#define OSTAGE(buf, p, LAST, LD2) do { const LAS char* S_ = L + sw; sw = nxt5(sw); const LAS char* N_ = L + sw; \
      OHALF(buf, 0, S_, S_ + 16384, (LAST) ? 0 : 2, true, !(LAST), (buf) ^ 1, 1, (p) + 1); \
      OHALF(buf, 1, S_ + 16384, N_, (LAST) ? 0 : 1, !(LAST), LD2, buf, 0, (p) + 2); } while (0)
    OLD4(0, 0, 0); OLD4(0, 0, 1); OLD4(1, 1, 0);
    { const LAS char* S_ = L + sw; ORD(0, S_); SBAR(); ORD(1, S_); ORD(2, S_); SBAR(); ORD(3, S_); SBAR(); ORD(4, S_); SBAR(); ORD(5, S_); SBAR(); }
#pragma unroll 1
    for (int p = 0; p < 6; p += 2) { OSTAGE(0, p, false, true); OSTAGE(1, p + 1, false, true); }
    OSTAGE(0, 6, false, false); OSTAGE(1, 7, true, false);
    STAMP(10);
    const int tile = ob * 8 + nb;
    GAS u64* flags2 = (GAS u64*)(ws + WS_FLAGS2) + (tile * 16 + qb) * 16;
    {
      const __amdgpu_buffer_rsrc_t rp = SRD(ws + WS_PART + (size_t)tile * 131072 + (kq * 4 + qb) * 2048, 131072);
#pragma unroll
      for (int i = 1; i < 4; ++i)
#pragma unroll
        for (int g = 0; g < 2; ++g) __builtin_amdgcn_raw_buffer_store_b128(__builtin_bit_cast(u32x4, pack8(acc[i], g)), rp, lane * 16 + ((kq + i) & 3) * 32768u + g * 1024, 0, SC1);
      WAITV(0);
      if (lane < 4) __hip_atomic_store(flags2 + lane * 64 + kq, tag, RLX_AGENT);
    }
    STAMP(4);
#pragma unroll 1
    for (unsigned spins = 0; spins < SPIN_CAP; ++spins) {
      if (__all(__hip_atomic_load(flags2 + kq * 64 + (lane & 3), RLX_AGENT) == tag || (lane & 3) == kq)) break;
      __builtin_amdgcn_s_sleep(1);
    }
    __builtin_amdgcn_fence(__ATOMIC_ACQUIRE, "wavefront");
    STAMP(5);
    {
      const __amdgpu_buffer_rsrc_t rp = SRD(ws + WS_PART + (size_t)tile * 131072 + kq * 32768 + qb * 2048, 32768);
      u32x4 pv[3][2];
#pragma unroll
      for (int i = 1; i < 4; ++i)
#pragma unroll
        for (int g = 0; g < 2; ++g) pv[i - 1][g] = __builtin_amdgcn_raw_buffer_load_b128(rp, lane * 16 + g * 1024, ((kq + i) & 3) * 8192u, SC1);
      f32x16 sum = acc[0];
#pragma unroll
      for (int i = 0; i < 3; ++i)
#pragma unroll
        for (int e = 0; e < 8; ++e) { const unsigned w = pv[i][e >> 2][e & 3]; sum[2 * e] += __uint_as_float(w << 16); sum[2 * e + 1] += __uint_as_float(w & 0xffff0000u); }
      GAS char* orow = (GAS char*)out + (size_t)(ob * 128 + qb * 32 + r) * (DM * 2) + (nb * 128 + kq * 32) * 2 + hh * 16;
#pragma unroll
      for (int g = 0; g < 2; ++g) {
        unsigned a0 = cvtpk(sum[8 * g], sum[8 * g + 1]), a1 = cvtpk(sum[8 * g + 2], sum[8 * g + 3]);
        unsigned b0 = cvtpk(sum[8 * g + 4], sum[8 * g + 5]), b1 = cvtpk(sum[8 * g + 6], sum[8 * g + 7]);
        auto r0 = __builtin_amdgcn_permlane32_swap(a0, b0, false, false);
        auto r1 = __builtin_amdgcn_permlane32_swap(a1, b1, false, false);
        u32x4 w = {r0[0], r1[0], r0[1], r1[1]};
        *(GAS u32x4*)(orow + g * 32) = w;
      }
    }
    STAMP(11);
  }
}

extern "C" void kernel_launch(void* const* d_in, const int* in_sizes, int n_in, void* d_out, int out_size, void* d_ws, size_t ws_size, hipStream_t stream) {
  mha_fused<<<256, 512, 0, stream>>>((const char*)d_in[0], (const char*)d_in[1], (const char*)d_in[2], (const char*)d_in[3], (const char*)d_in[4], (char*)d_out, (char*)d_ws);
}
```

```cpp
#include <hip/hip_runtime.h>
#include <hip/hip_bf16.h>

#define KV_B 2
#define CODE_KB 22

typedef __bf16 bf16x8 __attribute__((ext_vector_type(8)));
typedef __bf16 bf16x2 __attribute__((ext_vector_type(2)));
typedef short s16x4 __attribute__((ext_vector_type(4)));
typedef short s16x8 __attribute__((ext_vector_type(8)));
typedef float f32x16 __attribute__((ext_vector_type(16)));
typedef float f32x4 __attribute__((ext_vector_type(4)));
typedef float f32x2 __attribute__((ext_vector_type(2)));
typedef unsigned u32x4 __attribute__((ext_vector_type(4)));
typedef unsigned long long u64;
#define LAS __attribute__((address_space(3)))
#define GAS __attribute__((address_space(1)))
#define DI __device__ __forceinline__
#define MFMA(a, b, c) __builtin_amdgcn_mfma_f32_32x32x16_bf16((a), (b), (c), 0, 0, 0)
#define SBAR() __builtin_amdgcn_sched_barrier(0)
#define RLX_AGENT __ATOMIC_RELAXED, __HIP_MEMORY_SCOPE_AGENT
#define SRD(p, n) __builtin_amdgcn_make_buffer_rsrc((void*)(p), 0, (n), 0x00020000)

constexpr int SQ = 128, DM = 1024, NH = 32, SK = 1024, DH = 128;
constexpr int SLOT = 32768, LDS_BYTES = 163840;
constexpr float C2 = 0.08838834764831845f * 1.4426950408889634f;
constexpr float THR = 8.0f;
constexpr unsigned WS_FLAGS = 8u << 20, WS_EPOCH = WS_FLAGS + 65536u, WS_FLAGS2 = 9u << 20, WS_PART = 16u << 20;
constexpr u64 EPOCH_INC = 0x9E3779B97F4A7C15ull;
constexpr unsigned SPIN_CAP = 1u << 22;
constexpr int SC1 = 16, NT = 2;

#define STAMP(i) do {} while (0)
#define LS() do {} while (0)

DI unsigned rfl(unsigned x) { return __builtin_amdgcn_readfirstlane(x); }
DI unsigned cvtpk(float lo, float hi) { f32x2 v = {lo, hi}; bf16x2 b = __builtin_convertvector(v, bf16x2); return __builtin_bit_cast(unsigned, b); }
DI bf16x8 pack8(const f32x16& x, int s) {
  u32x4 w = {cvtpk(x[8 * s + 0], x[8 * s + 1]), cvtpk(x[8 * s + 2], x[8 * s + 3]), cvtpk(x[8 * s + 4], x[8 * s + 5]), cvtpk(x[8 * s + 6], x[8 * s + 7])};
  return __builtin_bit_cast(bf16x8, w);
}
DI void dma16(__amdgpu_buffer_rsrc_t rs, unsigned voff, unsigned soff, unsigned ldsaddr) {
  asm volatile("s_mov_b32 m0, %0\n\ts_nop 0\n\tbuffer_load_dwordx4 %1, %2, %3 offen lds" ::"s"(ldsaddr), "v"(voff), "s"(rs), "s"(soff) : "memory");
}
#define DMA_PC(V, NTS) "buffer_load_dwordx4 " V ", %4, %0 offen " NTS "lds\n\t"
#define DMA_2(VA, VB, NTS) DMA_PC(VA, NTS) "s_add_u32 m0, m0, 0x400\n\ts_add_u32 %0, %0, %5\n\t" DMA_PC(VB, NTS) "s_add_u32 m0, m0, 0x400\n\ts_add_u32 %0, %0, %6\n\t"
template <int SEL> DI void dma_tile(__amdgpu_buffer_rsrc_t rs, unsigned voe, unsigned voo, unsigned soff, unsigned p1, unsigned p2, unsigned ldsaddr) {
  const unsigned d2 = p2 - p1;
  if constexpr (SEL == 0)
    asm volatile("s_mov_b32 m0, %1\n\ts_nop 0\n\t" DMA_2("%2", "%3", "") DMA_2("%2", "%3", "") DMA_2("%2", "%3", "") DMA_2("%2", "%3", "") DMA_2("%2", "%3", "") DMA_2("%2", "%3", "") DMA_2("%2", "%3", "") DMA_2("%2", "%3", "")
                 : "+&s"(soff) : "s"(ldsaddr), "v"(voe), "v"(voo), "s"(rs), "s"(p1), "s"(d2) : "memory", "scc");
  else
    asm volatile("s_mov_b32 m0, %1\n\ts_nop 0\n\t" DMA_2("%2", "%2", "nt ") DMA_2("%3", "%3", "nt ") DMA_2("%2", "%2", "nt ") DMA_2("%3", "%3", "nt ") DMA_2("%2", "%2", "nt ") DMA_2("%3", "%3", "nt ") DMA_2("%2", "%2", "nt ") DMA_2("%3", "%3", "nt ")
                 : "+&s"(soff) : "s"(ldsaddr), "v"(voe), "v"(voo), "s"(rs), "s"(p1), "s"(d2) : "memory", "scc");
}
DI bf16x8 rd128(const LAS char* p) { return *(const LAS bf16x8*)p; }
DI bf16x8 rdtr(const LAS char* lo, const LAS char* up) {
  s16x4 a = __builtin_amdgcn_ds_read_tr16_b64_v4i16((LAS s16x4*)lo);
  s16x4 b = __builtin_amdgcn_ds_read_tr16_b64_v4i16((LAS s16x4*)up);
  s16x8 c = __builtin_shufflevector(a, b, 0, 1, 2, 3, 4, 5, 6, 7);
  return __builtin_bit_cast(bf16x8, c);
}
#define BAR() asm volatile("s_barrier" ::: "memory")
#define BAR_L() asm volatile("s_waitcnt lgkmcnt(0)\n\ts_barrier" ::: "memory")
#define WAITV(N) asm volatile("s_waitcnt vmcnt(" #N ")" ::: "memory")
#define WAITV_BAR(N) asm volatile("s_waitcnt vmcnt(" #N ")\n\ts_barrier" ::: "memory")
#define MX3(a, b, c) __builtin_fmaxf(__builtin_fmaxf((a), (b)), (c))
DI float both_max(float x) { auto r = __builtin_amdgcn_permlane32_swap(__float_as_uint(x), __float_as_uint(x), false, false); return __builtin_fmaxf(__uint_as_float(r[0]), __uint_as_float(r[1])); }
DI float both_sum(float x) { auto r = __builtin_amdgcn_permlane32_swap(__float_as_uint(x), __float_as_uint(x), false, false); return __uint_as_float(r[0]) + __uint_as_float(r[1]); }
DI void mfma_acc(f32x16& d, const bf16x8& a, const bf16x8& b) { asm volatile("v_mfma_f32_32x32x16_bf16 %0, %1, %2, %0" : "+v"(d) : "v"(a), "v"(b) : "memory"); }
DI void mfma_new(f32x16& d, const bf16x8& a, const bf16x8& b, const f32x16& c) { asm volatile("v_mfma_f32_32x32x16_bf16 %0, %1, %2, %3" : "=&v"(d) : "v"(a), "v"(b), "v"(c) : "memory"); }
DI float aexp(float x) { asm volatile("v_exp_f32 %0, %0" : "+v"(x) :: "memory"); return x; }
DI unsigned acvt(float a, float b) { unsigned r; asm volatile("v_cvt_pk_bf16_f32 %0, %1, %2" : "=v"(r) : "v"(a), "v"(b) : "memory"); return r; }
DI void aadd1(float& l, float a) { asm volatile("v_add_f32 %0, %0, %1" : "+v"(l) : "v"(a) : "memory"); }
DI void aadd2(float& l, float a, float b) { asm volatile("v_add_f32 %0, %0, %1\n\tv_add_f32 %0, %0, %2" : "+&v"(l) : "v"(a), "v"(b) : "memory"); }
DI void aadd3(float& l, float a, float b, float c) { asm volatile("v_add_f32 %0, %0, %1\n\tv_add_f32 %0, %0, %2\n\tv_add_f32 %0, %0, %3" : "+&v"(l) : "v"(a), "v"(b), "v"(c) : "memory"); }
DI void aadd4(float& l, float a, float b, float c, float d) { asm volatile("v_add_f32 %0, %0, %1\n\tv_add_f32 %0, %0, %2\n\tv_add_f32 %0, %0, %3\n\tv_add_f32 %0, %0, %4" : "+&v"(l) : "v"(a), "v"(b), "v"(c), "v"(d) : "memory"); }
DI float amax7(float a, float b, float c, float d, float e, float f, float g) { float r; asm volatile("v_max3_f32 %0, %1, %2, %3\n\tv_max3_f32 %0, %0, %4, %5\n\tv_max3_f32 %0, %0, %6, %7" : "=&v"(r) : "v"(a), "v"(b), "v"(c), "v"(d), "v"(e), "v"(f), "v"(g) : "memory"); return r; }
DI void amax6(float& r, float a, float b, float c, float d, float e, float f) { asm volatile("v_max3_f32 %0, %0, %1, %2\n\tv_max3_f32 %0, %0, %3, %4\n\tv_max3_f32 %0, %0, %5, %6" : "+&v"(r) : "v"(a), "v"(b), "v"(c), "v"(d), "v"(e), "v"(f) : "memory"); }
DI void amax8(float& r, float a, float b, float c, float d, float e, float f, float g, float h) { asm volatile("v_max3_f32 %0, %0, %1, %2\n\tv_max3_f32 %0, %0, %3, %4\n\tv_max3_f32 %0, %0, %5, %6\n\tv_max3_f32 %0, %0, %7, %8" : "+&v"(r) : "v"(a), "v"(b), "v"(c), "v"(d), "v"(e), "v"(f), "v"(g), "v"(h) : "memory"); }
DI void amax3z(float& r, float a, float b, float c) { asm volatile("v_max3_f32 %0, %0, %1, %2\n\tv_max3_f32 %0, %0, %3, 0" : "+&v"(r) : "v"(a), "v"(b), "v"(c) : "memory"); }
DI int nxt5(int s) { return s == 4 * SLOT ? 0 : s + SLOT; }
struct Tile { u32x4 v[16]; };
DI void tile_init(Tile& t) {
#pragma unroll
  for (int p = 0; p < 16; ++p) asm volatile("" : "=v"(t.v[p]));
}
template <int AUX, int IMM> DI void ld16(u32x4& d, __amdgpu_buffer_rsrc_t rs, unsigned voff, unsigned soff) {
  if constexpr (AUX == NT) asm volatile("buffer_load_dwordx4 %0, %1, %2, %3 offen offset:%4 nt" : "+v"(d) : "v"(voff), "s"(rs), "s"(soff), "i"(IMM) : "memory");
  else                     asm volatile("buffer_load_dwordx4 %0, %1, %2, %3 offen offset:%4" : "+v"(d) : "v"(voff), "s"(rs), "s"(soff), "i"(IMM) : "memory");
}
template <int P> DI void wr16(const u32x4& d, unsigned addr) { asm volatile("ds_write_b128 %0, %1 offset:%2" ::"v"(addr), "v"(d), "i"(P * 1024) : "memory"); }
DI void tile_wr(const Tile& t, unsigned addr) {
  wr16<0>(t.v[0], addr); wr16<1>(t.v[1], addr); wr16<2>(t.v[2], addr); wr16<3>(t.v[3], addr); wr16<4>(t.v[4], addr); wr16<5>(t.v[5], addr); wr16<6>(t.v[6], addr); wr16<7>(t.v[7], addr);
  wr16<8>(t.v[8], addr); wr16<9>(t.v[9], addr); wr16<10>(t.v[10], addr); wr16<11>(t.v[11], addr); wr16<12>(t.v[12], addr); wr16<13>(t.v[13], addr); wr16<14>(t.v[14], addr); wr16<15>(t.v[15], addr);
}

__global__ __launch_bounds__(512, 2) void mha_fused(const char* x, const char* kc, const char* vc, const char* wq, const char* wo, char* out, char* ws) {
  __shared__ __attribute__((aligned(16))) char lds[LDS_BYTES];
  u64 pc0; asm volatile("s_getpc_b64 %0" : "=s"(pc0));
  const int tid = threadIdx.x, lane = tid & 63;
  const int wid = rfl(tid >> 6);
  const int wg = blockIdx.x;
  const int xcd = wg & 7, j = wg >> 3;
  const int b = j & 7, h = xcd * 4 + (j >> 3);
  const bool late = (j ^ (j >> 3)) & 1;
  const int kq = xcd >> 1, ob = (xcd & 1) * 4 + (j >> 3), nb = j & 7;
  auto hmap = [&](int p) { return 8 * kq + 2 * (p & 3) + ((ob & 1) ^ (p >> 2)); };
  const unsigned lds0 = (unsigned)(size_t)(LAS char*)lds;

  const __amdgpu_buffer_rsrc_t rcode = SRD(pc0 & ~255ull, CODE_KB * 1024);
  if ((wid >> 1) != 2) dma16(rcode, lane * 16u, ((j & 3) * 6 + (wid < 4 ? wid : wid - 2)) * 1024u, lds0 + 4 * SLOT + wid * 1024);
  asm volatile("" : "+s"(x), "+s"(kc), "+s"(vc), "+s"(wq), "+s"(wo), "+s"(out), "+s"(ws));
  GAS u64* flags = (GAS u64*)(ws + WS_FLAGS);

  if (__builtin_expect(wid >= 4, 1)) {
    const unsigned r8 = (lane >> 2) & 7, sub = lane >> 5, c4 = lane & 3;
    const unsigned hb = wid & 1;
    const unsigned xe = hb ? c4 : (c4 ^ (r8 >> 2)), xo = hb ? c4 : (c4 ^ (2 + (r8 >> 2)));
    const unsigned ldh = lds0 + hb * 16384u;
    if (__builtin_expect(wid >= 6, 0)) {
      const __amdgpu_buffer_rsrc_t rs = SRD((hb ? vc : kc) + (size_t)(b * NH + h) * SK * DH * 2, SK * DH * 2);
      const unsigned voe = r8 * 256u + sub * 64u + 16u * xe, voo = r8 * 256u + sub * 64u + 16u * xo;
      const int krot = (j + 5 * xcd) & 15;
      auto tile_dma = [&](int t, int so) { dma_tile<1>(rs, voe, voo, ((t + krot) & 15) * 16384u, 128u, 2048u, ldh + so); };
      const __amdgpu_buffer_rsrc_t rwo = SRD(wo + nb * 256, NH * DH * DM * 2 - nb * 256);
      const unsigned vw0 = (hb * 64u + r8) * 2048u + 16u * c4 + 64u * ((kq + sub) & 3), vw1 = (hb * 64u + r8) * 2048u + 16u * c4 + 64u * ((kq + 2 + sub) & 3);
      auto w_dma = [&](int p, int so) { dma_tile<0>(rwo, vw0, vw1, (unsigned)hmap(p) * (128u * 2048u), 0u, 16384u, ldh + so); };
      WAITV(0);
      int sl = 3 * SLOT;
      int n0 = 5;
      if (late) {
        const unsigned wdst = ldh + lane * 16u;
        Tile R0, R1, R2;
        tile_init(R0); tile_init(R1); tile_init(R2);
        auto tile_ld = [&](Tile& R, int t) {
#pragma unroll
          for (int q = 0; q < 4; ++q) {
            const unsigned so = ((t + krot) & 15) * 16384u + q * 4096u;
            ld16<NT, 0>(R.v[4 * q], rs, voe, so); ld16<NT, 128>(R.v[4 * q + 1], rs, voe, so);
            ld16<NT, 2048>(R.v[4 * q + 2], rs, voo, so); ld16<NT, 2048 + 128>(R.v[4 * q + 3], rs, voo, so);
          }
        };
#pragma unroll 1
        for (int c = -1; c <= KV_B; ++c) BAR();
        tile_dma(0, 3 * SLOT); tile_dma(1, 4 * SLOT); tile_ld(R0, 2);
        WAITV(32); tile_ld(R1, 3);
        WAITV(32); tile_ld(R2, 4);
#pragma unroll 1
        for (int c = KV_B + 1; c <= 14; ++c) BAR();
        WAITV_BAR(0);
        tile_wr(R0, wdst);
        BAR_L(); tile_wr(R1, wdst + SLOT);
        if (hb) BAR_L();
        tile_wr(R2, wdst + 2 * SLOT);
        asm volatile("s_waitcnt lgkmcnt(0)" ::: "memory");
      } else {
#pragma unroll 1
        for (int c = -1; c <= 12 + (int)hb; ++c) BAR();
        __builtin_amdgcn_s_sleep(6);
        tile_dma(0, sl); sl = nxt5(sl);
        n0 = 1;
      }
#pragma unroll 1
      for (int n = n0; n <= 15; ++n) { WAITV_BAR(32); tile_dma(n, sl); sl = nxt5(sl); }
      if (!hb) {
        WAITV_BAR(32); WAITV_BAR(16); w_dma(0, sl); sl = nxt5(sl);
        WAITV_BAR(16); w_dma(1, sl); sl = nxt5(sl);
        BAR(); w_dma(2, sl); sl = nxt5(sl);
      } else {
#pragma unroll 1
        for (int s = 0; s <= 2; ++s) { WAITV_BAR(32); w_dma(s, sl); sl = nxt5(sl); }
      }
      WAITV_BAR(32);
      WAITV(31);
      __builtin_amdgcn_s_sleep(8);
      w_dma(3, sl); sl = nxt5(sl); w_dma(4, sl);
#pragma unroll 1
      for (int p = 5; p < 8; ++p) { BAR(); WAITV_BAR(47); sl = nxt5(sl); w_dma(p, sl); }
      BAR(); WAITV_BAR(47); BAR(); WAITV_BAR(32); BAR(); WAITV_BAR(16); BAR(); WAITV_BAR(0); BAR(); BAR();
      return;
    }
    u64 tag = 0;
    {
      const int qrot = (2 * (j & 7) + 4 * (j >> 3)) & 15;
      u64 qbase = hb ? (u64)wq : (u64)x; unsigned qoff = hb ? h * DH * 2 : b * SQ * DM * 2, qn = hb ? DM * NH * DH * 2 : (b + 1) * SQ * DM * 2;
      unsigned rsh = hb ? 13u : 11u, cstr = hb ? 64u * 8192u : 128u, p1 = hb ? 128u : 16384u, p2 = hb ? 65536u : 32768u;
      asm volatile("" : "+s"(qbase), "+s"(qoff), "+s"(qn), "+s"(rsh), "+s"(cstr), "+s"(p1), "+s"(p2));
      const __amdgpu_buffer_rsrc_t rs = SRD(qbase, qn);
      const unsigned voe = (r8 << rsh) + sub * 64u + 16u * xe, voo = (r8 << rsh) + sub * 64u + 16u * xo;
      int sl = 2 * SLOT;
      tag = __hip_atomic_load((GAS u64*)(ws + WS_EPOCH), RLX_AGENT);
#pragma unroll 1
      for (int c = 0; c < 16; ++c) {
        dma_tile<0>(rs, voe, voo, qoff + ((c + qrot) & 15) * cstr, p1, p2, ldh + sl); sl = sl == 2 * SLOT ? 0 : sl + SLOT;
        if (c == 2) { WAITV_BAR(32); LS(); }
        if (c >= 2) { WAITV_BAR(16); LS(); }
      }
      WAITV_BAR(0); LS(); BAR(); LS();
    }
    tag += EPOCH_INC;
    {
      u64 seen = 0;
      const GAS u64* myflag = flags + (((ob * 4 + kq) * 8 + nb) * 32 + (lane & 31));
      auto gate = [&](u64 need) {
        if (hb) return;
#pragma unroll 1
        for (unsigned spins = 0; (seen & need) != need && spins < SPIN_CAP; ++spins) {
          seen = __ballot(__hip_atomic_load(myflag, RLX_AGENT) == tag);
          if ((seen & need) != need) __builtin_amdgcn_s_sleep(1);
        }
      };
      auto hbit = [&](int p) { return 0x01010101ull << (hmap(p) & 7); };
#pragma unroll 1
      for (int t = 0; t <= 10; ++t) { BAR(); LS(); }
      u32x4 d0; asm volatile("" : "=v"(d0));
      ld16<0, 0>(d0, rcode, lane * 16u, ((j >> 1) * 2 + hb) * 1024u);
#pragma unroll 1
      for (int t = 11; t <= 14; ++t) { BAR(); LS(); }
      WAITV_BAR(0); LS();
      asm volatile("" ::"v"(d0));
      gate(hbit(0) | hbit(1)); BAR(); LS();
#pragma unroll 1
      for (int m = 0; m < 8; ++m) { if (m < 6) gate(hbit(m + 2)); BAR(); BAR(); LS(); }
    }
    if (wg == 0 && wid == 4) {
#pragma unroll 1
      for (unsigned spins = 0; spins < SPIN_CAP; ++spins) {
        bool ok = true;
#pragma unroll
        for (int i = 0; i < 16; ++i) ok &= __hip_atomic_load(flags + ((2 * i + (lane >> 5)) * 256 + (lane & 31)), RLX_AGENT) == tag;
        if (__all(ok)) break;
        __builtin_amdgcn_s_sleep(2);
      }
      if (lane == 0) __hip_atomic_store((GAS u64*)(ws + WS_EPOCH), tag, RLX_AGENT);
    }
    return;
  }
  STAMP(0);
  const u64 tag = __hip_atomic_load((GAS u64*)(ws + WS_EPOCH), RLX_AGENT) + EPOCH_INC;
  const int r = lane & 31, hh = lane >> 5, qb = wid;
  const int tq = (lane >> 2) & 3, tp = lane & 3, blk = (lane >> 4) & 1;
  const LAS char* L = (const LAS char*)lds;
  const int rowe = 64 * (r & 7) + 16 * (hh ^ ((r >> 2) & 3));
  const int kbase_e = 2048 * (r >> 3) + rowe, kbase_o = kbase_e ^ 32;
  const int tbase = hh * 256 + tq * 64 + blk * 32 + tp * 8;
  bf16x8 qf[8];
  bf16x8 kf[16], vf[16];
  f32x16 acc[4] = {};
#define KRD(i, SP) kf[i] = rd128((SP) + ((((i) >> 1) & 1) ? kbase_o : kbase_e) + 512 * ((i) >> 2) + 8192 * ((i) & 1))
  {
    const int xbase_e = 1024 * (4 * qb + (r >> 3)) + rowe, xbase_o = xbase_e ^ 32;
    const int pp = (tp == 1) ? 2 : (tp == 2) ? 1 : tp;
    const int wbase = 16384 + hh * 2048 + tq * 64 + blk * 32 + pp * 8;
    bf16x8 xf[4], wf[16];
    int sl = 2 * SLOT;
#define WRD(i, SP) wf[i] = rdtr((SP) + wbase + 4096 * ((i) >> 2) + 512 * ((i) & 3), (SP) + wbase + 4096 * ((i) >> 2) + 512 * ((i) & 3) + 256)
#define XRD(ks, SP) xf[ks] = rd128((SP) + (((ks) & 1) ? xbase_o : xbase_e) + 512 * ((ks) >> 1))
#define GQ(g, BODY) do { acc[(g) & 3] = MFMA(wf[g], xf[(g) >> 2], acc[(g) & 3]); BODY; SBAR(); } while (0)
    __builtin_amdgcn_s_waitcnt(0xC07F);
    WAITV_BAR(0);
    STAMP(1);
    { const LAS char* S = L + sl; XRD(0, S); WRD(0, S); SBAR(); WRD(1, S); WRD(2, S); SBAR(); WRD(3, S); XRD(1, S); SBAR(); WRD(4, S); SBAR(); WRD(5, S); SBAR(); }
#define QCHUNK(LAST) do { const LAS char* S = L + sl; sl = sl == 2 * SLOT ? 0 : sl + SLOT; const LAS char* N = (LAST) ? L + 3 * SLOT : L + sl; SBAR(); \
      GQ(0, { WRD(6, S); XRD(2, S); }); GQ(1, { WRD(7, S); XRD(3, S); }); GQ(2, { WRD(8, S); }); GQ(3, { WRD(9, S); WRD(10, S); }); GQ(4, WRD(11, S)); GQ(5, { WRD(12, S); WRD(13, S); }); \
      GQ(6, { WRD(14, S); }); GQ(7, WRD(15, S)); GQ(8, {}); GQ(9, {}); GQ(10, {}); \
      BAR_L(); SBAR(); GQ(11, {}); \
      if (!(LAST)) { XRD(0, N); WRD(0, N); SBAR(); GQ(12, { WRD(1, N); WRD(2, N); }); GQ(13, { WRD(3, N); XRD(1, N); }); GQ(14, { WRD(4, N); }); GQ(15, WRD(5, N)); } \
      else { GQ(12, { KRD(0, N); KRD(1, N); }); GQ(13, { KRD(2, N); KRD(3, N); }); GQ(14, { KRD(4, N); KRD(5, N); }); GQ(15, { KRD(6, N); KRD(7, N); }); } } while (0)
#pragma unroll 1
    for (int c = 0; c < 15; ++c) QCHUNK(false);
    QCHUNK(true);
#pragma unroll
    for (int d = 0; d < 4; ++d)
#pragma unroll
      for (int i = 0; i < 16; ++i) acc[d][i] *= C2;
#pragma unroll
    for (int s = 0; s < 8; ++s) qf[s] = pack8(acc[s >> 1], s & 1);
  }
  STAMP(2);
  {
    f32x16 o[4] = {};
    float l = 0.f;
    f32x16 sA0, sA1, sB0, sB1, negm;
    u32x4 pw[4];
    const f32x16 zero16 = {};
    int sk = 3 * SLOT;
    int sv = 3 * SLOT + 16384;
#define VRD(i, SP) vf[i] = rdtr((SP) + tbase + 4096 * ((i) >> 2) + 512 * ((i) & 3), (SP) + tbase + 4096 * ((i) >> 2) + 512 * ((i) & 3) + 2048)
#define EX(v) __builtin_amdgcn_exp2f(v)
#define PF(k) __builtin_bit_cast(bf16x8, pw[k])
#define LW(n) __builtin_amdgcn_s_waitcnt(0xC07F | ((n) << 8))
    {
      const LAS char* KP = L + sk;
#pragma unroll
      for (int i = 8; i < 16; ++i) KRD(i, KP);
      sA0 = MFMA(kf[0], qf[0], zero16); sA1 = MFMA(kf[1], qf[0], zero16);
#pragma unroll
      for (int s = 1; s < 8; ++s) { sA0 = MFMA(kf[2 * s], qf[s], sA0); sA1 = MFMA(kf[2 * s + 1], qf[s], sA1); }
      BAR_L();
      STAMP(3);
      sk = nxt5(sk);
      const LAS char* KN = L + sk;
      KRD(0, KN); SBAR(); KRD(1, KN); SBAR(); KRD(2, KN); SBAR(); KRD(3, KN); SBAR();
      float a = MX3(sA0[0], sA0[1], sA1[0]); a = MX3(a, sA1[1], sA0[2]);
#pragma unroll
      for (int i = 3; i < 16; i += 2) a = MX3(a, sA0[i], (i + 1 < 16) ? sA0[i + 1] : sA0[i]);
#pragma unroll
      for (int i = 2; i < 16; i += 2) a = MX3(a, sA1[i], sA1[i + 1]);
      const float m = both_max(a);
#pragma unroll
      for (int i = 0; i < 16; ++i) { sA0[i] -= m; sA1[i] -= m; negm[i] = -m; }
#pragma unroll
      for (int i = 0; i < 13; ++i) sA0[i] = EX(sA0[i]);
    }
#define GA(i, X, BODY) do { if ((i) < 2) mfma_new(X, kf[i], qf[0], negm); else mfma_acc(X, kf[i], qf[(i) >> 1]); if ((i) + 4 < 16) { KRD(((i) + 4) & 15, KP_); } else { VRD((i) & 3, VP_); } BODY; } while (0)
#define E1(Y, e) do { Y[e] = aexp(Y[e]); } while (0)
#define E2(Y, e) do { Y[e] = aexp(Y[e]); Y[e + 1] = aexp(Y[e + 1]); } while (0)
#define S1(Y, e) aadd1(l, Y[e])
#define S2(Y, e) aadd2(l, Y[e], Y[e + 1])
#define S3(Y, e) aadd3(l, Y[e], Y[e + 1], Y[e + 2])
#define S4(Y, e) aadd4(l, Y[e], Y[e + 1], Y[e + 2], Y[e + 3])
#define PK(k, d, Y, e) do { pw[k][d] = acvt(Y[e], Y[e + 1]); } while (0)
#define PHASE_A(X0, X1, Y0, Y1) do { const LAS char* KP_ = L + sk; const LAS char* VP_ = L + sv; \
      LW(2); GA(0,  X0, { E2(Y0, 13); }); \
             GA(1,  X1, { PK(0, 0, Y0, 0); E1(Y0, 15); E1(Y1, 0); }); \
      LW(2); GA(2,  X0, { E2(Y1, 1); }); \
             GA(3,  X1, { PK(0, 1, Y0, 2); E2(Y1, 3); }); \
      LW(2); GA(4,  X0, { E2(Y1, 5); asm volatile("" ::"v"(negm));   }); \
             GA(5,  X1, { PK(0, 2, Y0, 4); E2(Y1, 7); }); \
      LW(2); GA(6,  X0, { E2(Y1, 9); }); \
             GA(7,  X1, { PK(0, 3, Y0, 6); E2(Y1, 11); }); \
      LW(2); GA(8,  X0, { E2(Y1, 13); }); \
             GA(9,  X1, { PK(1, 0, Y0, 8); S2(Y0, 0); E1(Y1, 15); }); \
      LW(2); GA(10, X0, { PK(1, 1, Y0, 10); S3(Y0, 2); }); \
             GA(11, X1, { PK(1, 2, Y0, 12); S4(Y0, 5); }); \
      LW(2); GA(12, X0, { PK(1, 3, Y0, 14); S2(Y0, 9); }); \
             GA(13, X1, { S4(Y0, 11); }); \
      LW(4); GA(14, X0, { aadd3(l, Y0[15], Y1[0], Y1[1]); }); \
             GA(15, X1, { S2(Y1, 2); }); \
      } while (0)
#define GB(i, NEXTK, BODY) do { mfma_acc(o[(i) & 3], vf[i], PF((i) >> 2)); if ((i) + 4 < 16) { VRD(((i) + 4) & 15, VP_); } else if (NEXTK) { KRD((i) & 3, KN_); } BODY; } while (0)
#define PHASE_B(X0, X1, Y1, NEXTK) do { const LAS char* VP_ = L + sv; const LAS char* KN_ = L + sk; float a_; float alpha_ = 1.f; bool resc_ = false; \
      LW(4); GB(0, NEXTK, { PK(2, 0, Y1, 0); S2(Y1, 4); }); \
             GB(1, NEXTK, { PK(2, 1, Y1, 2); PK(2, 2, Y1, 4); PK(2, 3, Y1, 6); S1(Y1, 6); }); \
      LW(4); GB(2, NEXTK, { a_ = amax7(X0[0], X0[1], X0[2], X0[3], X0[4], X0[5], X0[6]); }); \
             GB(3, NEXTK, { amax8(a_, X0[7], X0[8], X0[9], X0[10], X0[11], X0[12], X0[13], X0[14]); }); \
      LW(4); GB(4, NEXTK, { amax6(a_, X0[15], X1[0], X1[1], X1[2], X1[3], X1[4]); }); \
             GB(5, NEXTK, { amax8(a_, X1[5], X1[6], X1[7], X1[8], X1[9], X1[10], X1[11], X1[12]); }); \
      LW(4); GB(6, NEXTK, { amax3z(a_, X1[13], X1[14], X1[15]); S1(Y1, 7); }); \
             GB(7, NEXTK, { S2(Y1, 8); }); \
      if (__builtin_expect(__any(a_ > THR), 0)) { const float dl_ = both_max(a_); alpha_ = __builtin_amdgcn_exp2f(-dl_); resc_ = true; \
        _Pragma("unroll") for (int i_ = 0; i_ < 16; ++i_) { X0[i_] -= dl_; X1[i_] -= dl_; negm[i_] -= dl_; } } \
      LW(4); GB(8,  NEXTK, { PK(3, 0, Y1, 8); PK(3, 1, Y1, 10); S1(Y1, 10); }); \
             GB(9,  NEXTK, { PK(3, 2, Y1, 12); PK(3, 3, Y1, 14); E1(X0, 0); }); \
      LW(4); GB(10, NEXTK, { S1(Y1, 11); E1(X0, 1); }); \
             GB(11, NEXTK, { E2(X0, 2); }); \
      LW(4); GB(12, NEXTK, { S1(Y1, 12); E2(X0, 4); }); \
             GB(13, NEXTK, { S1(Y1, 13); E2(X0, 6); }); \
      if (NEXTK) LW(2); else LW(0); \
             GB(14, NEXTK, { S1(Y1, 14); E2(X0, 8); }); \
             GB(15, NEXTK, { S1(Y1, 15); E2(X0, 10); E1(X0, 12); }); \
      if (__builtin_expect(resc_, 0)) { asm volatile("s_nop 7\n\ts_nop 7" : "+v"(o[0]), "+v"(o[1]), "+v"(o[2]), "+v"(o[3]) :: "memory"); \
        l *= alpha_; _Pragma("unroll") for (int d_ = 0; d_ < 4; ++d_) _Pragma("unroll") for (int i_ = 0; i_ < 16; ++i_) o[d_][i_] *= alpha_; } \
      sv = sv == 4 * SLOT + 16384 ? 16384 : sv + SLOT; } while (0)
    asm volatile("s_nop 1" : "+v"(negm), "+v"(sA0), "+v"(sA1) :: "memory");
#pragma unroll 1
    for (int t = 1; t < 15; t += 2) {
      PHASE_A(sB0, sB1, sA0, sA1); BAR(); sk = nxt5(sk); PHASE_B(sB0, sB1, sA1, true);
      PHASE_A(sA0, sA1, sB0, sB1); BAR(); sk = nxt5(sk); PHASE_B(sA0, sA1, sB1, true);
    }
    PHASE_A(sB0, sB1, sA0, sA1); BAR(); PHASE_B(sB0, sB1, sA1, false);
    {
      const LAS char* VP_ = L + sv;
#pragma unroll
      for (int i = 0; i < 16; ++i) VRD(i, VP_);
#pragma unroll
      for (int i = 13; i < 16; ++i) sB0[i] = EX(sB0[i]);
      const bf16x8 p0 = pack8(sB0, 0), p1 = pack8(sB0, 1);
#pragma unroll
      for (int db = 0; db < 4; ++db) o[db] = MFMA(vf[db], p0, o[db]);
#pragma unroll
      for (int i = 0; i < 16; ++i) sB1[i] = EX(sB1[i]);
#pragma unroll
      for (int db = 0; db < 4; ++db) o[db] = MFMA(vf[4 + db], p1, o[db]);
      const bf16x8 p2 = pack8(sB1, 0), p3 = pack8(sB1, 1);
#pragma unroll
      for (int db = 0; db < 4; ++db) o[db] = MFMA(vf[8 + db], p2, o[db]);
#pragma unroll
      for (int i = 0; i < 16; ++i) l += sB0[i];
#pragma unroll
      for (int db = 0; db < 4; ++db) o[db] = MFMA(vf[12 + db], p3, o[db]);
#pragma unroll
      for (int i = 0; i < 16; ++i) l += sB1[i];
      l = both_sum(l);
    }
    STAMP(7);
    const float inv = __builtin_amdgcn_rcpf(l);
    const __amdgpu_buffer_rsrc_t rs = SRD(ws + (unsigned)(((b * 4 + qb) * 256 + h * 8) * 1024), 8192);
#pragma unroll
    for (int db = 0; db < 4; ++db) {
#pragma unroll
      for (int i = 0; i < 16; ++i) o[db][i] *= inv;
#pragma unroll
      for (int s = 0; s < 2; ++s) __builtin_amdgcn_raw_buffer_store_b128(__builtin_bit_cast(u32x4, pack8(o[db], s)), rs, lane * 16 + (db * 2 + s) * 1024, 0, SC1);
    }
#pragma unroll
    for (int i = 0; i < 4; ++i) acc[i] = (f32x16){};
    asm volatile("s_waitcnt vmcnt(0)" : "+v"(acc[0]), "+v"(acc[1]), "+v"(acc[2]), "+v"(acc[3]) :: "memory");
    if (lane < 8) __hip_atomic_store(flags + (((b * 4 + (h >> 3)) * 8 + lane) * 32 + qb * 8 + (h & 7)), tag, RLX_AGENT);
  }
  STAMP(8);
  {
    BAR_L();
    STAMP(9);
    const __amdgpu_buffer_rsrc_t rso = SRD(ws + (size_t)(ob * 4 + qb) * 262144, 262144);
    u32x4 bq[2][8];
    bf16x8 wf[16];
    int sw = 4 * SLOT;
#define OL1(lb, lh, so_, i_) bq[lb][4 * (lh) + (i_)] = __builtin_amdgcn_raw_buffer_load_b128(rso, lane * 16 + (i_) * 1024, so_, SC1)
#define OLD4(buf, p, hf) do { const unsigned so_ = (unsigned)hmap(p) * 8192u + (hf) * 4096u; _Pragma("unroll") for (int i_ = 0; i_ < 4; ++i_) { OL1(buf, hf, so_, i_); SBAR(); } } while (0)
#define ORD(i, SP) wf[i] = rdtr((SP) + tbase + 4096 * ((i) >> 2) + 512 * ((i) & 3), (SP) + tbase + 4096 * ((i) >> 2) + 512 * ((i) & 3) + 2048)
#define GO(buf, hf, g, BODY) do { acc[(g) & 3] = MFMA(wf[g], __builtin_bit_cast(bf16x8, bq[buf][4 * (hf) + ((g) >> 2)]), acc[(g) & 3]); BODY; SBAR(); } while (0)
#define OHALF(buf, hf, S, N, SYNC, MORE, LD, lb, lh, lp) do { unsigned so_ = 0; SBAR(); \
      GO(buf, hf, 0, { ORD(6, S); so_ = (unsigned)hmap(lp) * 8192u + (lh) * 4096u + (unsigned)((lp) >> 3) * 0x40000000u; asm volatile("" : "+s"(so_)); }); GO(buf, hf, 1, { ORD(7, S); if (LD) OL1(lb, lh, so_, 0); }); \
      GO(buf, hf, 2, { ORD(8, S); if (LD) OL1(lb, lh, so_, 1); }); GO(buf, hf, 3, { ORD(9, S); if (LD) OL1(lb, lh, so_, 2); }); GO(buf, hf, 4, { ORD(10, S); if (LD) OL1(lb, lh, so_, 3); }); \
      GO(buf, hf, 5, { ORD(11, S); ORD(12, S); }); GO(buf, hf, 6, { ORD(13, S); ORD(14, S); }); GO(buf, hf, 7, ORD(15, S)); GO(buf, hf, 8, {}); GO(buf, hf, 9, {}); GO(buf, hf, 10, {}); \
      if ((SYNC) == 1) { BAR_L(); SBAR(); } else if ((SYNC) == 2) { BAR(); SBAR(); } \
      GO(buf, hf, 11, if (MORE) ORD(0, N)); GO(buf, hf, 12, if (MORE) { ORD(1, N); ORD(2, N); }); GO(buf, hf, 13, if (MORE) ORD(3, N)); GO(buf, hf, 14, if (MORE) ORD(4, N)); GO(buf, hf, 15, if (MORE) ORD(5, N)); } while (0)
#define OSTAGE(buf, p, LAST, LD2) do { const LAS char* S_ = L + sw; sw = nxt5(sw); const LAS char* N_ = L + sw; \
      OHALF(buf, 0, S_, S_ + 16384, (LAST) ? 0 : 2, true, !(LAST), (buf) ^ 1, 1, (p) + 1); \
      OHALF(buf, 1, S_ + 16384, N_, (LAST) ? 0 : 1, !(LAST), LD2, buf, 0, (p) + 2); } while (0)
    OLD4(0, 0, 0); OLD4(0, 0, 1); OLD4(1, 1, 0);
    { const LAS char* S_ = L + sw; ORD(0, S_); SBAR(); ORD(1, S_); ORD(2, S_); SBAR(); ORD(3, S_); SBAR(); ORD(4, S_); SBAR(); ORD(5, S_); SBAR(); }
#pragma unroll 1
    for (int p = 0; p < 8; p += 2) { OSTAGE(0, p, false, true); OSTAGE(1, p + 1, false, true); }
    STAMP(10);
    const int tile = ob * 8 + nb;
    GAS u64* flags2 = (GAS u64*)(ws + WS_FLAGS2) + (tile * 16 + qb) * 16;
    {
      const __amdgpu_buffer_rsrc_t rp = SRD(ws + WS_PART + (size_t)tile * 131072 + (kq * 4 + qb) * 2048, 131072);
#pragma unroll
      for (int i = 1; i < 4; ++i)
#pragma unroll
        for (int g = 0; g < 2; ++g) __builtin_amdgcn_raw_buffer_store_b128(__builtin_bit_cast(u32x4, pack8(acc[i], g)), rp, lane * 16 + ((kq + i) & 3) * 32768u + g * 1024, 0, SC1);
      WAITV(0);
      if (lane < 4) __hip_atomic_store(flags2 + lane * 64 + kq, tag, RLX_AGENT);
    }
    STAMP(4);
#pragma unroll 1
    for (unsigned spins = 0; spins < SPIN_CAP; ++spins) {
      if (__all(__hip_atomic_load(flags2 + kq * 64 + (lane & 3), RLX_AGENT) == tag || (lane & 3) == kq)) break;
      __builtin_amdgcn_s_sleep(1);
    }
    __builtin_amdgcn_fence(__ATOMIC_ACQUIRE, "wavefront");
    STAMP(5);
    {
      const __amdgpu_buffer_rsrc_t rp = SRD(ws + WS_PART + (size_t)tile * 131072 + kq * 32768 + qb * 2048, 32768);
      u32x4 pv[3][2];
#pragma unroll
      for (int i = 1; i < 4; ++i)
#pragma unroll
        for (int g = 0; g < 2; ++g) pv[i - 1][g] = __builtin_amdgcn_raw_buffer_load_b128(rp, lane * 16 + g * 1024, ((kq + i) & 3) * 8192u, SC1);
      f32x16 sum = acc[0];
#pragma unroll
      for (int i = 0; i < 3; ++i)
#pragma unroll
        for (int e = 0; e < 8; ++e) { const unsigned w = pv[i][e >> 2][e & 3]; sum[2 * e] += __uint_as_float(w << 16); sum[2 * e + 1] += __uint_as_float(w & 0xffff0000u); }
      GAS char* orow = (GAS char*)out + (size_t)(ob * 128 + qb * 32 + r) * (DM * 2) + (nb * 128 + kq * 32) * 2 + hh * 16;
#pragma unroll
      for (int g = 0; g < 2; ++g) {
        unsigned a0 = cvtpk(sum[8 * g], sum[8 * g + 1]), a1 = cvtpk(sum[8 * g + 2], sum[8 * g + 3]);
        unsigned b0 = cvtpk(sum[8 * g + 4], sum[8 * g + 5]), b1 = cvtpk(sum[8 * g + 6], sum[8 * g + 7]);
        auto r0 = __builtin_amdgcn_permlane32_swap(a0, b0, false, false);
        auto r1 = __builtin_amdgcn_permlane32_swap(a1, b1, false, false);
        u32x4 w = {r0[0], r1[0], r0[1], r1[1]};
        *(GAS u32x4*)(orow + g * 32) = w;
      }
    }
    STAMP(11);
  }
}

extern "C" void kernel_launch(void* const* d_in, const int* in_sizes, int n_in, void* d_out, int out_size, void* d_ws, size_t ws_size, hipStream_t stream) {
  mha_fused<<<256, 512, 0, stream>>>((const char*)d_in[0], (const char*)d_in[1], (const char*)d_in[2], (const char*)d_in[3], (const char*)d_in[4], (char*)d_out, (char*)d_ws);
}
```

```cpp
#include <hip/hip_runtime.h>
#include <hip/hip_bf16.h>

#define KV_B -1
#define XA 2
#define XA0 0
#define BW 12
#define BWAIT() WAITV(12)
#define CODE_KB 23

typedef __bf16 bf16x8 __attribute__((ext_vector_type(8)));
typedef __bf16 bf16x2 __attribute__((ext_vector_type(2)));
typedef short s16x4 __attribute__((ext_vector_type(4)));
typedef short s16x8 __attribute__((ext_vector_type(8)));
typedef float f32x16 __attribute__((ext_vector_type(16)));
typedef float f32x4 __attribute__((ext_vector_type(4)));
typedef float f32x2 __attribute__((ext_vector_type(2)));
typedef unsigned u32x4 __attribute__((ext_vector_type(4)));
typedef unsigned long long u64;
#define LAS __attribute__((address_space(3)))
#define GAS __attribute__((address_space(1)))
#define DI __device__ __forceinline__
#define MFMA(a, b, c) __builtin_amdgcn_mfma_f32_32x32x16_bf16((a), (b), (c), 0, 0, 0)
#define SBAR() __builtin_amdgcn_sched_barrier(0)
#define RLX_AGENT __ATOMIC_RELAXED, __HIP_MEMORY_SCOPE_AGENT
#define SRD(p, n) __builtin_amdgcn_make_buffer_rsrc((void*)(p), 0, (n), 0x00020000)

constexpr int SQ = 128, DM = 1024, NH = 32, SK = 1024, DH = 128;
constexpr int SLOT = 32768, LDS_BYTES = 163840;
constexpr float C2 = 0.08838834764831845f * 1.4426950408889634f;
constexpr float THR = 8.0f;
constexpr unsigned WS_FLAGS = 8u << 20, WS_EPOCH = WS_FLAGS + 65536u, WS_FLAGS2 = 9u << 20, WS_PART = 16u << 20;
constexpr u64 EPOCH_INC = 0x9E3779B97F4A7C15ull;
constexpr unsigned SPIN_CAP = 1u << 22;
constexpr int SC1 = 16, NT = 2;

#define STAMP(i) do {} while (0)
#define LS() do {} while (0)

DI unsigned rfl(unsigned x) { return __builtin_amdgcn_readfirstlane(x); }
DI unsigned cvtpk(float lo, float hi) { f32x2 v = {lo, hi}; bf16x2 b = __builtin_convertvector(v, bf16x2); return __builtin_bit_cast(unsigned, b); }
DI bf16x8 pack8(const f32x16& x, int s) {
  u32x4 w = {cvtpk(x[8 * s + 0], x[8 * s + 1]), cvtpk(x[8 * s + 2], x[8 * s + 3]), cvtpk(x[8 * s + 4], x[8 * s + 5]), cvtpk(x[8 * s + 6], x[8 * s + 7])};
  return __builtin_bit_cast(bf16x8, w);
}
DI void dma16(__amdgpu_buffer_rsrc_t rs, unsigned voff, unsigned soff, unsigned ldsaddr) {
  asm volatile("s_mov_b32 m0, %0\n\ts_nop 0\n\tbuffer_load_dwordx4 %1, %2, %3 offen lds" ::"s"(ldsaddr), "v"(voff), "s"(rs), "s"(soff) : "memory");
}
#define DMA_PC(V, NTS) "buffer_load_dwordx4 " V ", %4, %0 offen " NTS "lds\n\t"
#define DMA_2(VA, VB, NTS) DMA_PC(VA, NTS) "s_add_u32 m0, m0, 0x400\n\ts_add_u32 %0, %0, %5\n\t" DMA_PC(VB, NTS) "s_add_u32 m0, m0, 0x400\n\ts_add_u32 %0, %0, %6\n\t"
template <int SEL> DI void dma_tile(__amdgpu_buffer_rsrc_t rs, unsigned voe, unsigned voo, unsigned soff, unsigned p1, unsigned p2, unsigned ldsaddr) {
  const unsigned d2 = p2 - p1;
  if constexpr (SEL == 0)
    asm volatile("s_mov_b32 m0, %1\n\ts_nop 0\n\t" DMA_2("%2", "%3", "") DMA_2("%2", "%3", "") DMA_2("%2", "%3", "") DMA_2("%2", "%3", "") DMA_2("%2", "%3", "") DMA_2("%2", "%3", "") DMA_2("%2", "%3", "") DMA_2("%2", "%3", "")
                 : "+&s"(soff) : "s"(ldsaddr), "v"(voe), "v"(voo), "s"(rs), "s"(p1), "s"(d2) : "memory", "scc");
  else
    asm volatile("s_mov_b32 m0, %1\n\ts_nop 0\n\t" DMA_2("%2", "%2", "nt ") DMA_2("%3", "%3", "nt ") DMA_2("%2", "%2", "nt ") DMA_2("%3", "%3", "nt ") DMA_2("%2", "%2", "nt ") DMA_2("%3", "%3", "nt ") DMA_2("%2", "%2", "nt ") DMA_2("%3", "%3", "nt ")
                 : "+&s"(soff) : "s"(ldsaddr), "v"(voe), "v"(voo), "s"(rs), "s"(p1), "s"(d2) : "memory", "scc");
}
DI bf16x8 rd128(const LAS char* p) { return *(const LAS bf16x8*)p; }
DI bf16x8 rdtr(const LAS char* lo, const LAS char* up) {
  s16x4 a = __builtin_amdgcn_ds_read_tr16_b64_v4i16((LAS s16x4*)lo);
  s16x4 b = __builtin_amdgcn_ds_read_tr16_b64_v4i16((LAS s16x4*)up);
  s16x8 c = __builtin_shufflevector(a, b, 0, 1, 2, 3, 4, 5, 6, 7);
  return __builtin_bit_cast(bf16x8, c);
}
#define BAR() asm volatile("s_barrier" ::: "memory")
#define BAR_L() asm volatile("s_waitcnt lgkmcnt(0)\n\ts_barrier" ::: "memory")
#define WAITV(N) asm volatile("s_waitcnt vmcnt(" #N ")" ::: "memory")
#define WAITV_BAR(N) asm volatile("s_waitcnt vmcnt(" #N ")\n\ts_barrier" ::: "memory")
#define MX3(a, b, c) __builtin_fmaxf(__builtin_fmaxf((a), (b)), (c))
DI float both_max(float x) { auto r = __builtin_amdgcn_permlane32_swap(__float_as_uint(x), __float_as_uint(x), false, false); return __builtin_fmaxf(__uint_as_float(r[0]), __uint_as_float(r[1])); }
DI float both_sum(float x) { auto r = __builtin_amdgcn_permlane32_swap(__float_as_uint(x), __float_as_uint(x), false, false); return __uint_as_float(r[0]) + __uint_as_float(r[1]); }
DI void mfma_acc(f32x16& d, const bf16x8& a, const bf16x8& b) { asm volatile("v_mfma_f32_32x32x16_bf16 %0, %1, %2, %0" : "+v"(d) : "v"(a), "v"(b) : "memory"); }
DI void mfma_new(f32x16& d, const bf16x8& a, const bf16x8& b, const f32x16& c) { asm volatile("v_mfma_f32_32x32x16_bf16 %0, %1, %2, %3" : "=&v"(d) : "v"(a), "v"(b), "v"(c) : "memory"); }
DI float aexp(float x) { asm volatile("v_exp_f32 %0, %0" : "+v"(x) :: "memory"); return x; }
DI unsigned acvt(float a, float b) { unsigned r; asm volatile("v_cvt_pk_bf16_f32 %0, %1, %2" : "=v"(r) : "v"(a), "v"(b) : "memory"); return r; }
DI void aadd1(float& l, float a) { asm volatile("v_add_f32 %0, %0, %1" : "+v"(l) : "v"(a) : "memory"); }
DI void aadd2(float& l, float a, float b) { asm volatile("v_add_f32 %0, %0, %1\n\tv_add_f32 %0, %0, %2" : "+&v"(l) : "v"(a), "v"(b) : "memory"); }
DI void aadd3(float& l, float a, float b, float c) { asm volatile("v_add_f32 %0, %0, %1\n\tv_add_f32 %0, %0, %2\n\tv_add_f32 %0, %0, %3" : "+&v"(l) : "v"(a), "v"(b), "v"(c) : "memory"); }
DI void aadd4(float& l, float a, float b, float c, float d) { asm volatile("v_add_f32 %0, %0, %1\n\tv_add_f32 %0, %0, %2\n\tv_add_f32 %0, %0, %3\n\tv_add_f32 %0, %0, %4" : "+&v"(l) : "v"(a), "v"(b), "v"(c), "v"(d) : "memory"); }
DI float amax7(float a, float b, float c, float d, float e, float f, float g) { float r; asm volatile("v_max3_f32 %0, %1, %2, %3\n\tv_max3_f32 %0, %0, %4, %5\n\tv_max3_f32 %0, %0, %6, %7" : "=&v"(r) : "v"(a), "v"(b), "v"(c), "v"(d), "v"(e), "v"(f), "v"(g) : "memory"); return r; }
DI void amax6(float& r, float a, float b, float c, float d, float e, float f) { asm volatile("v_max3_f32 %0, %0, %1, %2\n\tv_max3_f32 %0, %0, %3, %4\n\tv_max3_f32 %0, %0, %5, %6" : "+&v"(r) : "v"(a), "v"(b), "v"(c), "v"(d), "v"(e), "v"(f) : "memory"); }
DI void amax8(float& r, float a, float b, float c, float d, float e, float f, float g, float h) { asm volatile("v_max3_f32 %0, %0, %1, %2\n\tv_max3_f32 %0, %0, %3, %4\n\tv_max3_f32 %0, %0, %5, %6\n\tv_max3_f32 %0, %0, %7, %8" : "+&v"(r) : "v"(a), "v"(b), "v"(c), "v"(d), "v"(e), "v"(f), "v"(g), "v"(h) : "memory"); }
DI void amax3z(float& r, float a, float b, float c) { asm volatile("v_max3_f32 %0, %0, %1, %2\n\tv_max3_f32 %0, %0, %3, 0" : "+&v"(r) : "v"(a), "v"(b), "v"(c) : "memory"); }
DI int nxt5(int s) { return s == 4 * SLOT ? 0 : s + SLOT; }
struct Tile { u32x4 v[16]; };
DI void tile_init(Tile& t) {
#pragma unroll
  for (int p = 0; p < 16; ++p) asm volatile("" : "=v"(t.v[p]));
}
template <int AUX, int IMM> DI void ld16(u32x4& d, __amdgpu_buffer_rsrc_t rs, unsigned voff, unsigned soff) {
  if constexpr (AUX == NT) asm volatile("buffer_load_dwordx4 %0, %1, %2, %3 offen offset:%4 nt" : "+v"(d) : "v"(voff), "s"(rs), "s"(soff), "i"(IMM) : "memory");
  else                     asm volatile("buffer_load_dwordx4 %0, %1, %2, %3 offen offset:%4" : "+v"(d) : "v"(voff), "s"(rs), "s"(soff), "i"(IMM) : "memory");
}
template <int P> DI void wr16(const u32x4& d, unsigned addr) { asm volatile("ds_write_b128 %0, %1 offset:%2" ::"v"(addr), "v"(d), "i"(P * 1024) : "memory"); }
DI void tile_wr(const Tile& t, unsigned addr) {
  wr16<0>(t.v[0], addr); wr16<1>(t.v[1], addr); wr16<2>(t.v[2], addr); wr16<3>(t.v[3], addr); wr16<4>(t.v[4], addr); wr16<5>(t.v[5], addr); wr16<6>(t.v[6], addr); wr16<7>(t.v[7], addr);
  wr16<8>(t.v[8], addr); wr16<9>(t.v[9], addr); wr16<10>(t.v[10], addr); wr16<11>(t.v[11], addr); wr16<12>(t.v[12], addr); wr16<13>(t.v[13], addr); wr16<14>(t.v[14], addr); wr16<15>(t.v[15], addr);
}

__global__ __launch_bounds__(512, 2) void mha_fused(const char* x, const char* kc, const char* vc, const char* wq, const char* wo, char* out, char* ws) {
  __shared__ __attribute__((aligned(16))) char lds[LDS_BYTES];
  u64 pc0; asm volatile("s_getpc_b64 %0" : "=s"(pc0));
  const int tid = threadIdx.x, lane = tid & 63;
  const int wid = rfl(tid >> 6);
  const int wg = blockIdx.x;
  const int xcd = wg & 7, j = wg >> 3;
  const int b = j & 7, h = xcd * 4 + (j >> 3);
  const bool late = (j ^ (j >> 3)) & 1;
  const int kq = xcd >> 1, ob = (xcd & 1) * 4 + (j >> 3), nb = j & 7;
  auto hmap = [&](int p) { return 8 * kq + 2 * (p & 3) + ((ob & 1) ^ (p >> 2)); };
  const unsigned lds0 = (unsigned)(size_t)(LAS char*)lds;

  const __amdgpu_buffer_rsrc_t rcode = SRD(pc0 & ~255ull, CODE_KB * 1024);
  if ((wid >> 1) != 2) dma16(rcode, lane * 16u, ((j & 3) * 6 + (wid < 4 ? wid : wid - 2)) * 1024u, lds0 + 4 * SLOT + wid * 1024);
  asm volatile("" : "+s"(x), "+s"(kc), "+s"(vc), "+s"(wq), "+s"(wo), "+s"(out), "+s"(ws));
  GAS u64* flags = (GAS u64*)(ws + WS_FLAGS);

  if (__builtin_expect(wid >= 4, 1)) {
    const unsigned r8 = (lane >> 2) & 7, sub = lane >> 5, c4 = lane & 3;
    const unsigned hb = wid & 1;
    const unsigned xe = hb ? c4 : (c4 ^ (r8 >> 2)), xo = hb ? c4 : (c4 ^ (2 + (r8 >> 2)));
    const unsigned ldh = lds0 + hb * 16384u;
#define KV_SETUP() const __amdgpu_buffer_rsrc_t rkv = SRD((hb ? vc : kc) + (size_t)(b * NH + h) * SK * DH * 2, SK * DH * 2); \
    const unsigned kve = r8 * 256u + sub * 64u + 16u * xe, kvo = r8 * 256u + sub * 64u + 16u * xo; \
    const int krot = (j + 5 * xcd) & 15;     \
    const unsigned wdst = ldh + lane * 16u; \
    auto tile_ld4 = [&](Tile& R, int t, int q) { const unsigned so = ((t + krot) & 15) * 16384u + q * 4096u; \
      ld16<NT, 0>(R.v[4 * q], rkv, kve, so); ld16<NT, 128>(R.v[4 * q + 1], rkv, kve, so); ld16<NT, 2048>(R.v[4 * q + 2], rkv, kvo, so); ld16<NT, 2048 + 128>(R.v[4 * q + 3], rkv, kvo, so); }
    if (__builtin_expect(wid >= 6, 0)) {
      KV_SETUP();
      auto tile_ld = [&](Tile& R, int t) { tile_ld4(R, t, 0); BWAIT(); tile_ld4(R, t, 1); BWAIT(); tile_ld4(R, t, 2); BWAIT(); tile_ld4(R, t, 3); BWAIT(); };
      auto tile_dma = [&](int t, int so) { dma_tile<1>(rkv, kve, kvo, ((t + krot) & 15) * 16384u, 128u, 2048u, ldh + so); };
      const __amdgpu_buffer_rsrc_t rwo = SRD(wo + nb * 256, NH * DH * DM * 2 - nb * 256);
      const unsigned vw0 = (hb * 64u + r8) * 2048u + 16u * c4 + 64u * ((kq + sub) & 3), vw1 = (hb * 64u + r8) * 2048u + 16u * c4 + 64u * ((kq + 2 + sub) & 3);
      auto w_dma = [&](int p, int so) { dma_tile<0>(rwo, vw0, vw1, (unsigned)hmap(p) * (128u * 2048u), 0u, 16384u, ldh + so); };
      WAITV(0);
      int sl = 3 * SLOT;
      int n0 = 5;
      if (late) {
        Tile R0, R1, R2;
        tile_init(R0); tile_init(R1); tile_init(R2);
#pragma unroll 1
        for (int c = -1; c <= KV_B; ++c) BAR();
        tile_dma(0, 3 * SLOT); BWAIT(); tile_dma(1, 4 * SLOT); BWAIT(); tile_ld(R0, 2);
        WAITV(32); tile_ld(R1, 3);
        WAITV(32); tile_ld(R2, 4);
#pragma unroll 1
        for (int c = KV_B + 1; c <= 14; ++c) BAR();
        WAITV_BAR(0);
        tile_wr(R0, wdst);
        BAR_L(); tile_wr(R1, wdst + SLOT);
        if (hb) BAR_L();
        tile_wr(R2, wdst + 2 * SLOT);
        asm volatile("s_waitcnt lgkmcnt(0)" ::: "memory");
      } else {
#pragma unroll 1
        for (int c = -1; c <= 12 + (int)hb; ++c) BAR();
        __builtin_amdgcn_s_sleep(6);
        tile_dma(0, sl); sl = nxt5(sl);
        n0 = 1;
      }
#pragma unroll 1
      for (int n = n0; n <= ((XA && !late) ? 12 : 15); ++n) { WAITV_BAR(32); tile_dma(n, sl); sl = nxt5(sl); }
      if (XA && !late) { WAITV_BAR(32); WAITV_BAR(16); WAITV_BAR(0); sl = 4 * SLOT; }
      if (!hb) {
        WAITV_BAR(32); WAITV_BAR(16); w_dma(0, sl); sl = nxt5(sl);
        WAITV_BAR(16); w_dma(1, sl); sl = nxt5(sl);
        BAR(); w_dma(2, sl); sl = nxt5(sl);
      } else {
#pragma unroll 1
        for (int s = 0; s <= 2; ++s) { WAITV_BAR(32); w_dma(s, sl); sl = nxt5(sl); }
      }
      WAITV_BAR(32);
      WAITV(31);
      __builtin_amdgcn_s_sleep(8);
      w_dma(3, sl); sl = nxt5(sl); w_dma(4, sl);
#pragma unroll 1
      for (int p = 5; p < 8; ++p) { BAR(); WAITV_BAR(47); sl = nxt5(sl); w_dma(p, sl); }
      BAR(); WAITV_BAR(47); BAR(); WAITV_BAR(32); BAR(); WAITV_BAR(16); BAR(); WAITV_BAR(0); BAR(); BAR();
      return;
    }
    u64 tag = 0;
    {
      const int qrot = (2 * (j & 7) + 4 * (j >> 3)) & 15;
      u64 qbase = hb ? (u64)wq : (u64)x; unsigned qoff = hb ? h * DH * 2 : b * SQ * DM * 2, qn = hb ? DM * NH * DH * 2 : (b + 1) * SQ * DM * 2;
      unsigned rsh = hb ? 13u : 11u, cstr = hb ? 64u * 8192u : 128u, p1 = hb ? 128u : 16384u, p2 = hb ? 65536u : 32768u;
      asm volatile("" : "+s"(qbase), "+s"(qoff), "+s"(qn), "+s"(rsh), "+s"(cstr), "+s"(p1), "+s"(p2));
      const __amdgpu_buffer_rsrc_t rs = SRD(qbase, qn);
      const unsigned voe = (r8 << rsh) + sub * 64u + 16u * xe, voo = (r8 << rsh) + sub * 64u + 16u * xo;
      int sl = 2 * SLOT;
      tag = __hip_atomic_load((GAS u64*)(ws + WS_EPOCH), RLX_AGENT);
#pragma unroll 1
      for (int c = 0; c < 16; ++c) {
        dma_tile<0>(rs, voe, voo, qoff + ((c + qrot) & 15) * cstr, p1, p2, ldh + sl); sl = sl == 2 * SLOT ? 0 : sl + SLOT;
        if (c == 2) { WAITV_BAR(32); LS(); }
        if (c >= 2) { WAITV_BAR(16); LS(); }
      }
      WAITV_BAR(0); LS(); BAR(); LS();
    }
    tag += EPOCH_INC;
    {
      u64 seen = 0;
      const GAS u64* myflag = flags + (((ob * 4 + kq) * 8 + nb) * 32 + (lane & 31));
      auto gate = [&](u64 need) {
        if (hb) return;
#pragma unroll 1
        for (unsigned spins = 0; (seen & need) != need && spins < SPIN_CAP; ++spins) {
          seen = __ballot(__hip_atomic_load(myflag, RLX_AGENT) == tag);
          if ((seen & need) != need) __builtin_amdgcn_s_sleep(1);
        }
      };
      auto hbit = [&](int p) { return 0x01010101ull << (hmap(p) & 7); };
      if (XA && !late) {
        KV_SETUP();
        Tile R0, R1, R2;
        tile_init(R0); tile_init(R1); tile_init(R2);
#pragma unroll 1
        for (int t = 0; t < XA0; ++t) { BAR(); LS(); }
        BAR(); LS(); tile_ld4(R0, 13, 0); tile_ld4(R0, 13, 1);
        BAR(); LS(); tile_ld4(R0, 13, 2); tile_ld4(R0, 13, 3);
        BAR(); LS(); tile_ld4(R1, 14, 0); tile_ld4(R1, 14, 1);
        BAR(); LS(); tile_ld4(R1, 14, 2); tile_ld4(R1, 14, 3);
        BAR(); LS(); tile_ld4(R2, 15, 0); tile_ld4(R2, 15, 1);
        BAR(); LS(); tile_ld4(R2, 15, 2); tile_ld4(R2, 15, 3);
#pragma unroll 1
        for (int t = XA0 + 6; t <= 9; ++t) { BAR(); LS(); }
        WAITV_BAR(0); LS(); tile_wr(R0, wdst + SLOT);
        BAR_L(); LS(); tile_wr(R1, wdst + 2 * SLOT);
        ld16<0, 0>(R0.v[0], rcode, lane * 16u, ((j >> 1) * 2 + hb) * 1024u);
        BAR_L(); LS(); tile_wr(R2, wdst + 3 * SLOT);
        BAR_L(); LS(); WAITV_BAR(0); LS();
        asm volatile("" ::"v"(R0.v[0]));
      } else {
#pragma unroll 1
        for (int t = 0; t <= 10; ++t) { BAR(); LS(); }
        u32x4 d0; asm volatile("" : "=v"(d0));
        ld16<0, 0>(d0, rcode, lane * 16u, ((j >> 1) * 2 + hb) * 1024u);
        BAR(); LS(); BAR(); LS(); BAR(); LS(); WAITV_BAR(0); LS();
        asm volatile("" ::"v"(d0));
      }
      BAR(); LS();
      gate(hbit(0) | hbit(1)); BAR(); LS();
#pragma unroll 1
      for (int m = 0; m < 8; ++m) { if (m < 6) gate(hbit(m + 2)); BAR(); BAR(); LS(); }
    }
    if (wg == 0 && wid == 4) {
#pragma unroll 1
      for (unsigned spins = 0; spins < SPIN_CAP; ++spins) {
        bool ok = true;
#pragma unroll
        for (int i = 0; i < 16; ++i) ok &= __hip_atomic_load(flags + ((2 * i + (lane >> 5)) * 256 + (lane & 31)), RLX_AGENT) == tag;
        if (__all(ok)) break;
        __builtin_amdgcn_s_sleep(2);
      }
      if (lane == 0) __hip_atomic_store((GAS u64*)(ws + WS_EPOCH), tag, RLX_AGENT);
    }
    return;
  }
  STAMP(0);
  const u64 tag = __hip_atomic_load((GAS u64*)(ws + WS_EPOCH), RLX_AGENT) + EPOCH_INC;
  const int r = lane & 31, hh = lane >> 5, qb = wid;
  const int tq = (lane >> 2) & 3, tp = lane & 3, blk = (lane >> 4) & 1;
  const LAS char* L = (const LAS char*)lds;
  const int rowe = 64 * (r & 7) + 16 * (hh ^ ((r >> 2) & 3));
  const int kbase_e = 2048 * (r >> 3) + rowe, kbase_o = kbase_e ^ 32;
  const int tbase = hh * 256 + tq * 64 + blk * 32 + tp * 8;
  bf16x8 qf[8];
  bf16x8 kf[16], vf[16];
  f32x16 acc[4] = {};
#define KRD(i, SP) kf[i] = rd128((SP) + ((((i) >> 1) & 1) ? kbase_o : kbase_e) + 512 * ((i) >> 2) + 8192 * ((i) & 1))
  {
    const int xbase_e = 1024 * (4 * qb + (r >> 3)) + rowe, xbase_o = xbase_e ^ 32;
    const int pp = (tp == 1) ? 2 : (tp == 2) ? 1 : tp;
    const int wbase = 16384 + hh * 2048 + tq * 64 + blk * 32 + pp * 8;
    bf16x8 xf[4], wf[16];
    int sl = 2 * SLOT;
#define WRD(i, SP) wf[i] = rdtr((SP) + wbase + 4096 * ((i) >> 2) + 512 * ((i) & 3), (SP) + wbase + 4096 * ((i) >> 2) + 512 * ((i) & 3) + 256)
#define XRD(ks, SP) xf[ks] = rd128((SP) + (((ks) & 1) ? xbase_o : xbase_e) + 512 * ((ks) >> 1))
#define GQ(g, BODY) do { acc[(g) & 3] = MFMA(wf[g], xf[(g) >> 2], acc[(g) & 3]); BODY; SBAR(); } while (0)
    __builtin_amdgcn_s_waitcnt(0xC07F);
    WAITV_BAR(0);
    STAMP(1);
    { const LAS char* S = L + sl; XRD(0, S); WRD(0, S); SBAR(); WRD(1, S); WRD(2, S); SBAR(); WRD(3, S); XRD(1, S); SBAR(); WRD(4, S); SBAR(); WRD(5, S); SBAR(); }
#define QCHUNK(LAST) do { const LAS char* S = L + sl; sl = sl == 2 * SLOT ? 0 : sl + SLOT; const LAS char* N = (LAST) ? L + 3 * SLOT : L + sl; SBAR(); \
      GQ(0, { WRD(6, S); XRD(2, S); }); GQ(1, { WRD(7, S); XRD(3, S); }); GQ(2, { WRD(8, S); }); GQ(3, { WRD(9, S); WRD(10, S); }); GQ(4, WRD(11, S)); GQ(5, { WRD(12, S); WRD(13, S); }); \
      GQ(6, { WRD(14, S); }); GQ(7, WRD(15, S)); GQ(8, {}); GQ(9, {}); GQ(10, {}); \
      BAR_L(); SBAR(); GQ(11, {}); \
      if (!(LAST)) { XRD(0, N); WRD(0, N); SBAR(); GQ(12, { WRD(1, N); WRD(2, N); }); GQ(13, { WRD(3, N); XRD(1, N); }); GQ(14, { WRD(4, N); }); GQ(15, WRD(5, N)); } \
      else { GQ(12, { KRD(0, N); KRD(1, N); }); GQ(13, { KRD(2, N); KRD(3, N); }); GQ(14, { KRD(4, N); KRD(5, N); }); GQ(15, { KRD(6, N); KRD(7, N); }); } } while (0)
#pragma unroll 1
    for (int c = 0; c < 15; ++c) QCHUNK(false);
    QCHUNK(true);
#pragma unroll
    for (int d = 0; d < 4; ++d)
#pragma unroll
      for (int i = 0; i < 16; ++i) acc[d][i] *= C2;
#pragma unroll
    for (int s = 0; s < 8; ++s) qf[s] = pack8(acc[s >> 1], s & 1);
  }
  STAMP(2);
  {
    f32x16 o[4] = {};
    float l = 0.f;
    f32x16 sA0, sA1, sB0, sB1, negm;
    u32x4 pw[4];
    const f32x16 zero16 = {};
    int sk = 3 * SLOT;
    int sv = 3 * SLOT + 16384;
#define VRD(i, SP) vf[i] = rdtr((SP) + tbase + 4096 * ((i) >> 2) + 512 * ((i) & 3), (SP) + tbase + 4096 * ((i) >> 2) + 512 * ((i) & 3) + 2048)
#define EX(v) __builtin_amdgcn_exp2f(v)
#define PF(k) __builtin_bit_cast(bf16x8, pw[k])
#define LW(n) __builtin_amdgcn_s_waitcnt(0xC07F | ((n) << 8))
    {
      const LAS char* KP = L + sk;
#pragma unroll
      for (int i = 8; i < 16; ++i) KRD(i, KP);
      sA0 = MFMA(kf[0], qf[0], zero16); sA1 = MFMA(kf[1], qf[0], zero16);
#pragma unroll
      for (int s = 1; s < 8; ++s) { sA0 = MFMA(kf[2 * s], qf[s], sA0); sA1 = MFMA(kf[2 * s + 1], qf[s], sA1); }
      BAR_L();
      STAMP(3);
      sk = nxt5(sk);
      const LAS char* KN = L + sk;
      KRD(0, KN); SBAR(); KRD(1, KN); SBAR(); KRD(2, KN); SBAR(); KRD(3, KN); SBAR();
      float a = MX3(sA0[0], sA0[1], sA1[0]); a = MX3(a, sA1[1], sA0[2]);
#pragma unroll
      for (int i = 3; i < 16; i += 2) a = MX3(a, sA0[i], (i + 1 < 16) ? sA0[i + 1] : sA0[i]);
#pragma unroll
      for (int i = 2; i < 16; i += 2) a = MX3(a, sA1[i], sA1[i + 1]);
      const float m = both_max(a);
#pragma unroll
      for (int i = 0; i < 16; ++i) { sA0[i] -= m; sA1[i] -= m; negm[i] = -m; }
#pragma unroll
      for (int i = 0; i < 13; ++i) sA0[i] = EX(sA0[i]);
    }
#define GA(i, X, BODY) do { if ((i) < 2) mfma_new(X, kf[i], qf[0], negm); else mfma_acc(X, kf[i], qf[(i) >> 1]); if ((i) + 4 < 16) { KRD(((i) + 4) & 15, KP_); } else { VRD((i) & 3, VP_); } BODY; } while (0)
#define E1(Y, e) do { Y[e] = aexp(Y[e]); } while (0)
#define E2(Y, e) do { Y[e] = aexp(Y[e]); Y[e + 1] = aexp(Y[e + 1]); } while (0)
#define S1(Y, e) aadd1(l, Y[e])
#define S2(Y, e) aadd2(l, Y[e], Y[e + 1])
#define S3(Y, e) aadd3(l, Y[e], Y[e + 1], Y[e + 2])
#define S4(Y, e) aadd4(l, Y[e], Y[e + 1], Y[e + 2], Y[e + 3])
#define PK(k, d, Y, e) do { pw[k][d] = acvt(Y[e], Y[e + 1]); } while (0)
#define PHASE_A(X0, X1, Y0, Y1) do { const LAS char* KP_ = L + sk; const LAS char* VP_ = L + sv; \
      LW(2); GA(0,  X0, { E2(Y0, 13); }); \
             GA(1,  X1, { PK(0, 0, Y0, 0); E1(Y0, 15); E1(Y1, 0); }); \
      LW(2); GA(2,  X0, { E2(Y1, 1); }); \
             GA(3,  X1, { PK(0, 1, Y0, 2); E2(Y1, 3); }); \
      LW(2); GA(4,  X0, { E2(Y1, 5); asm volatile("" ::"v"(negm));   }); \
             GA(5,  X1, { PK(0, 2, Y0, 4); E2(Y1, 7); }); \
      LW(2); GA(6,  X0, { E2(Y1, 9); }); \
             GA(7,  X1, { PK(0, 3, Y0, 6); E2(Y1, 11); }); \
      LW(2); GA(8,  X0, { E2(Y1, 13); }); \
             GA(9,  X1, { PK(1, 0, Y0, 8); S2(Y0, 0); E1(Y1, 15); }); \
      LW(2); GA(10, X0, { PK(1, 1, Y0, 10); S3(Y0, 2); }); \
             GA(11, X1, { PK(1, 2, Y0, 12); S4(Y0, 5); }); \
      LW(2); GA(12, X0, { PK(1, 3, Y0, 14); S2(Y0, 9); }); \
             GA(13, X1, { S4(Y0, 11); }); \
      LW(4); GA(14, X0, { aadd3(l, Y0[15], Y1[0], Y1[1]); }); \
             GA(15, X1, { S2(Y1, 2); }); \
      } while (0)
#define GB(i, NEXTK, BODY) do { mfma_acc(o[(i) & 3], vf[i], PF((i) >> 2)); if ((i) + 4 < 16) { VRD(((i) + 4) & 15, VP_); } else if (NEXTK) { KRD((i) & 3, KN_); } BODY; } while (0)
#define PHASE_B(X0, X1, Y1, NEXTK) do { const LAS char* VP_ = L + sv; const LAS char* KN_ = L + sk; float a_; float alpha_ = 1.f; bool resc_ = false; \
      LW(4); GB(0, NEXTK, { PK(2, 0, Y1, 0); S2(Y1, 4); }); \
             GB(1, NEXTK, { PK(2, 1, Y1, 2); PK(2, 2, Y1, 4); PK(2, 3, Y1, 6); S1(Y1, 6); }); \
      LW(4); GB(2, NEXTK, { a_ = amax7(X0[0], X0[1], X0[2], X0[3], X0[4], X0[5], X0[6]); }); \
             GB(3, NEXTK, { amax8(a_, X0[7], X0[8], X0[9], X0[10], X0[11], X0[12], X0[13], X0[14]); }); \
      LW(4); GB(4, NEXTK, { amax6(a_, X0[15], X1[0], X1[1], X1[2], X1[3], X1[4]); }); \
             GB(5, NEXTK, { amax8(a_, X1[5], X1[6], X1[7], X1[8], X1[9], X1[10], X1[11], X1[12]); }); \
      LW(4); GB(6, NEXTK, { amax3z(a_, X1[13], X1[14], X1[15]); S1(Y1, 7); }); \
             GB(7, NEXTK, { S2(Y1, 8); }); \
      if (__builtin_expect(__any(a_ > THR), 0)) { const float dl_ = both_max(a_); alpha_ = __builtin_amdgcn_exp2f(-dl_); resc_ = true; \
        _Pragma("unroll") for (int i_ = 0; i_ < 16; ++i_) { X0[i_] -= dl_; X1[i_] -= dl_; negm[i_] -= dl_; } } \
      LW(4); GB(8,  NEXTK, { PK(3, 0, Y1, 8); PK(3, 1, Y1, 10); S1(Y1, 10); }); \
             GB(9,  NEXTK, { PK(3, 2, Y1, 12); PK(3, 3, Y1, 14); E1(X0, 0); }); \
      LW(4); GB(10, NEXTK, { S1(Y1, 11); E1(X0, 1); }); \
             GB(11, NEXTK, { E2(X0, 2); }); \
      LW(4); GB(12, NEXTK, { S1(Y1, 12); E2(X0, 4); }); \
             GB(13, NEXTK, { S1(Y1, 13); E2(X0, 6); }); \
      if (NEXTK) LW(2); else LW(0); \
             GB(14, NEXTK, { S1(Y1, 14); E2(X0, 8); }); \
             GB(15, NEXTK, { S1(Y1, 15); E2(X0, 10); E1(X0, 12); }); \
      if (__builtin_expect(resc_, 0)) { asm volatile("s_nop 7\n\ts_nop 7" : "+v"(o[0]), "+v"(o[1]), "+v"(o[2]), "+v"(o[3]) :: "memory"); \
        l *= alpha_; _Pragma("unroll") for (int d_ = 0; d_ < 4; ++d_) _Pragma("unroll") for (int i_ = 0; i_ < 16; ++i_) o[d_][i_] *= alpha_; } \
      sv = sv == 4 * SLOT + 16384 ? 16384 : sv + SLOT; } while (0)
    asm volatile("s_nop 1" : "+v"(negm), "+v"(sA0), "+v"(sA1) :: "memory");
#pragma unroll 1
    for (int t = 1; t < 15; t += 2) {
      PHASE_A(sB0, sB1, sA0, sA1); BAR(); sk = nxt5(sk); PHASE_B(sB0, sB1, sA1, true);
      PHASE_A(sA0, sA1, sB0, sB1); BAR(); sk = nxt5(sk); PHASE_B(sA0, sA1, sB1, true);
    }
    PHASE_A(sB0, sB1, sA0, sA1); BAR(); PHASE_B(sB0, sB1, sA1, false);
    {
      const LAS char* VP_ = L + sv;
#pragma unroll
      for (int i = 0; i < 16; ++i) VRD(i, VP_);
#pragma unroll
      for (int i = 13; i < 16; ++i) sB0[i] = EX(sB0[i]);
      const bf16x8 p0 = pack8(sB0, 0), p1 = pack8(sB0, 1);
#pragma unroll
      for (int db = 0; db < 4; ++db) o[db] = MFMA(vf[db], p0, o[db]);
#pragma unroll
      for (int i = 0; i < 16; ++i) sB1[i] = EX(sB1[i]);
#pragma unroll
      for (int db = 0; db < 4; ++db) o[db] = MFMA(vf[4 + db], p1, o[db]);
      const bf16x8 p2 = pack8(sB1, 0), p3 = pack8(sB1, 1);
#pragma unroll
      for (int db = 0; db < 4; ++db) o[db] = MFMA(vf[8 + db], p2, o[db]);
#pragma unroll
      for (int i = 0; i < 16; ++i) l += sB0[i];
#pragma unroll
      for (int db = 0; db < 4; ++db) o[db] = MFMA(vf[12 + db], p3, o[db]);
#pragma unroll
      for (int i = 0; i < 16; ++i) l += sB1[i];
      l = both_sum(l);
    }
    STAMP(7);
    const float inv = __builtin_amdgcn_rcpf(l);
    const __amdgpu_buffer_rsrc_t rs = SRD(ws + (unsigned)(((b * 4 + qb) * 256 + h * 8) * 1024), 8192);
#pragma unroll
    for (int db = 0; db < 4; ++db) {
#pragma unroll
      for (int i = 0; i < 16; ++i) o[db][i] *= inv;
#pragma unroll
      for (int s = 0; s < 2; ++s) __builtin_amdgcn_raw_buffer_store_b128(__builtin_bit_cast(u32x4, pack8(o[db], s)), rs, lane * 16 + (db * 2 + s) * 1024, 0, SC1);
    }
#pragma unroll
    for (int i = 0; i < 4; ++i) acc[i] = (f32x16){};
    asm volatile("s_waitcnt vmcnt(0)" : "+v"(acc[0]), "+v"(acc[1]), "+v"(acc[2]), "+v"(acc[3]) :: "memory");
    if (lane < 8) __hip_atomic_store(flags + (((b * 4 + (h >> 3)) * 8 + lane) * 32 + qb * 8 + (h & 7)), tag, RLX_AGENT);
  }
  STAMP(8);
  {
    BAR_L();
    STAMP(9);
    const __amdgpu_buffer_rsrc_t rso = SRD(ws + (size_t)(ob * 4 + qb) * 262144, 262144);
    u32x4 bq[2][8];
    bf16x8 wf[16];
    int sw = 4 * SLOT;
#define OL1(lb, lh, so_, i_) bq[lb][4 * (lh) + (i_)] = __builtin_amdgcn_raw_buffer_load_b128(rso, lane * 16 + (i_) * 1024, so_, SC1)
#define OLD4(buf, p, hf) do { const unsigned so_ = (unsigned)hmap(p) * 8192u + (hf) * 4096u; _Pragma("unroll") for (int i_ = 0; i_ < 4; ++i_) { OL1(buf, hf, so_, i_); SBAR(); } } while (0)
#define ORD(i, SP) wf[i] = rdtr((SP) + tbase + 4096 * ((i) >> 2) + 512 * ((i) & 3), (SP) + tbase + 4096 * ((i) >> 2) + 512 * ((i) & 3) + 2048)
#define GO(buf, hf, g, BODY) do { acc[(g) & 3] = MFMA(wf[g], __builtin_bit_cast(bf16x8, bq[buf][4 * (hf) + ((g) >> 2)]), acc[(g) & 3]); BODY; SBAR(); } while (0)
#define OHALF(buf, hf, S, N, SYNC, MORE, LD, lb, lh, lp) do { unsigned so_ = 0; SBAR(); \
      GO(buf, hf, 0, { ORD(6, S); so_ = (unsigned)hmap(lp) * 8192u + (lh) * 4096u + (unsigned)((lp) >> 3) * 0x40000000u; asm volatile("" : "+s"(so_)); }); GO(buf, hf, 1, { ORD(7, S); if (LD) OL1(lb, lh, so_, 0); }); \
      GO(buf, hf, 2, { ORD(8, S); if (LD) OL1(lb, lh, so_, 1); }); GO(buf, hf, 3, { ORD(9, S); if (LD) OL1(lb, lh, so_, 2); }); GO(buf, hf, 4, { ORD(10, S); if (LD) OL1(lb, lh, so_, 3); }); \
      GO(buf, hf, 5, { ORD(11, S); ORD(12, S); }); GO(buf, hf, 6, { ORD(13, S); ORD(14, S); }); GO(buf, hf, 7, ORD(15, S)); GO(buf, hf, 8, {}); GO(buf, hf, 9, {}); GO(buf, hf, 10, {}); \
      if ((SYNC) == 1) { BAR_L(); SBAR(); } else if ((SYNC) == 2) { BAR(); SBAR(); } \
      GO(buf, hf, 11, if (MORE) ORD(0, N)); GO(buf, hf, 12, if (MORE) { ORD(1, N); ORD(2, N); }); GO(buf, hf, 13, if (MORE) ORD(3, N)); GO(buf, hf, 14, if (MORE) ORD(4, N)); GO(buf, hf, 15, if (MORE) ORD(5, N)); } while (0)
#define OSTAGE(buf, p, LAST, LD2) do { const LAS char* S_ = L + sw; sw = nxt5(sw); const LAS char* N_ = L + sw; \
      OHALF(buf, 0, S_, S_ + 16384, (LAST) ? 0 : 2, true, !(LAST), (buf) ^ 1, 1, (p) + 1); \
      OHALF(buf, 1, S_ + 16384, N_, (LAST) ? 0 : 1, !(LAST), LD2, buf, 0, (p) + 2); } while (0)
    OLD4(0, 0, 0); OLD4(0, 0, 1); OLD4(1, 1, 0);
    { const LAS char* S_ = L + sw; ORD(0, S_); SBAR(); ORD(1, S_); ORD(2, S_); SBAR(); ORD(3, S_); SBAR(); ORD(4, S_); SBAR(); ORD(5, S_); SBAR(); }
#pragma unroll 1
    for (int p = 0; p < 8; p += 2) { OSTAGE(0, p, false, true); OSTAGE(1, p + 1, false, true); }
    STAMP(10);
    const int tile = ob * 8 + nb;
    GAS u64* flags2 = (GAS u64*)(ws + WS_FLAGS2) + (tile * 16 + qb) * 16;
    {
      const __amdgpu_buffer_rsrc_t rp = SRD(ws + WS_PART + (size_t)tile * 131072 + (kq * 4 + qb) * 2048, 131072);
#pragma unroll
      for (int i = 1; i < 4; ++i)
#pragma unroll
        for (int g = 0; g < 2; ++g) __builtin_amdgcn_raw_buffer_store_b128(__builtin_bit_cast(u32x4, pack8(acc[i], g)), rp, lane * 16 + ((kq + i) & 3) * 32768u + g * 1024, 0, SC1);
      WAITV(0);
      if (lane < 4) __hip_atomic_store(flags2 + lane * 64 + kq, tag, RLX_AGENT);
    }
    STAMP(4);
#pragma unroll 1
    for (unsigned spins = 0; spins < SPIN_CAP; ++spins) {
      if (__all(__hip_atomic_load(flags2 + kq * 64 + (lane & 3), RLX_AGENT) == tag || (lane & 3) == kq)) break;
      __builtin_amdgcn_s_sleep(1);
    }
    __builtin_amdgcn_fence(__ATOMIC_ACQUIRE, "wavefront");
    STAMP(5);
    {
      const __amdgpu_buffer_rsrc_t rp = SRD(ws + WS_PART + (size_t)tile * 131072 + kq * 32768 + qb * 2048, 32768);
      u32x4 pv[3][2];
#pragma unroll
      for (int i = 1; i < 4; ++i)
#pragma unroll
        for (int g = 0; g < 2; ++g) pv[i - 1][g] = __builtin_amdgcn_raw_buffer_load_b128(rp, lane * 16 + g * 1024, ((kq + i) & 3) * 8192u, SC1);
      f32x16 sum = acc[0];
#pragma unroll
      for (int i = 0; i < 3; ++i)
#pragma unroll
        for (int e = 0; e < 8; ++e) { const unsigned w = pv[i][e >> 2][e & 3]; sum[2 * e] += __uint_as_float(w << 16); sum[2 * e + 1] += __uint_as_float(w & 0xffff0000u); }
      GAS char* orow = (GAS char*)out + (size_t)(ob * 128 + qb * 32 + r) * (DM * 2) + (nb * 128 + kq * 32) * 2 + hh * 16;
#pragma unroll
      for (int g = 0; g < 2; ++g) {
        unsigned a0 = cvtpk(sum[8 * g], sum[8 * g + 1]), a1 = cvtpk(sum[8 * g + 2], sum[8 * g + 3]);
        unsigned b0 = cvtpk(sum[8 * g + 4], sum[8 * g + 5]), b1 = cvtpk(sum[8 * g + 6], sum[8 * g + 7]);
        auto r0 = __builtin_amdgcn_permlane32_swap(a0, b0, false, false);
        auto r1 = __builtin_amdgcn_permlane32_swap(a1, b1, false, false);
        u32x4 w = {r0[0], r1[0], r0[1], r1[1]};
        *(GAS u32x4*)(orow + g * 32) = w;
      }
    }
    STAMP(11);
  }
}

extern "C" void kernel_launch(void* const* d_in, const int* in_sizes, int n_in, void* d_out, int out_size, void* d_ws, size_t ws_size, hipStream_t stream) {
  mha_fused<<<256, 512, 0, stream>>>((const char*)d_in[0], (const char*)d_in[1], (const char*)d_in[2], (const char*)d_in[3], (const char*)d_in[4], (char*)d_out, (char*)d_ws);
}
```

```cpp
#include <hip/hip_runtime.h>
#include <hip/hip_bf16.h>

#define KV_B -1
#define XA 0
#define XA0 0
#define BW 0
#define BWAIT() do {} while (0)
#define CODE_KB 23

typedef __bf16 bf16x8 __attribute__((ext_vector_type(8)));
typedef __bf16 bf16x2 __attribute__((ext_vector_type(2)));
typedef short s16x4 __attribute__((ext_vector_type(4)));
typedef short s16x8 __attribute__((ext_vector_type(8)));
typedef float f32x16 __attribute__((ext_vector_type(16)));
typedef float f32x4 __attribute__((ext_vector_type(4)));
typedef float f32x2 __attribute__((ext_vector_type(2)));
typedef unsigned u32x4 __attribute__((ext_vector_type(4)));
typedef unsigned long long u64;
#define LAS __attribute__((address_space(3)))
#define GAS __attribute__((address_space(1)))
#define DI __device__ __forceinline__
#define MFMA(a, b, c) __builtin_amdgcn_mfma_f32_32x32x16_bf16((a), (b), (c), 0, 0, 0)
#define SBAR() __builtin_amdgcn_sched_barrier(0)
#define RLX_AGENT __ATOMIC_RELAXED, __HIP_MEMORY_SCOPE_AGENT
#define SRD(p, n) __builtin_amdgcn_make_buffer_rsrc((void*)(p), 0, (n), 0x00020000)

constexpr int SQ = 128, DM = 1024, NH = 32, SK = 1024, DH = 128;
constexpr int SLOT = 32768, LDS_BYTES = 163840;
constexpr float C2 = 0.08838834764831845f * 1.4426950408889634f;
constexpr float THR = 8.0f;
constexpr unsigned WS_FLAGS = 8u << 20, WS_EPOCH = WS_FLAGS + 65536u, WS_FLAGS2 = 9u << 20, WS_PART = 16u << 20;
constexpr u64 EPOCH_INC = 0x9E3779B97F4A7C15ull;
constexpr unsigned SPIN_CAP = 1u << 22;
constexpr int SC1 = 16, NT = 2;

#define STAMP(i) do {} while (0)
#define LS() do {} while (0)

DI unsigned rfl(unsigned x) { return __builtin_amdgcn_readfirstlane(x); }
DI unsigned cvtpk(float lo, float hi) { f32x2 v = {lo, hi}; bf16x2 b = __builtin_convertvector(v, bf16x2); return __builtin_bit_cast(unsigned, b); }
DI bf16x8 pack8(const f32x16& x, int s) {
  u32x4 w = {cvtpk(x[8 * s + 0], x[8 * s + 1]), cvtpk(x[8 * s + 2], x[8 * s + 3]), cvtpk(x[8 * s + 4], x[8 * s + 5]), cvtpk(x[8 * s + 6], x[8 * s + 7])};
  return __builtin_bit_cast(bf16x8, w);
}
DI void dma16(__amdgpu_buffer_rsrc_t rs, unsigned voff, unsigned soff, unsigned ldsaddr) {
  asm volatile("s_mov_b32 m0, %0\n\ts_nop 0\n\tbuffer_load_dwordx4 %1, %2, %3 offen lds" ::"s"(ldsaddr), "v"(voff), "s"(rs), "s"(soff) : "memory");
}
#define DMA_PC(V, NTS) "buffer_load_dwordx4 " V ", %4, %0 offen " NTS "lds\n\t"
#define DMA_2(VA, VB, NTS) DMA_PC(VA, NTS) "s_add_u32 m0, m0, 0x400\n\ts_add_u32 %0, %0, %5\n\t" DMA_PC(VB, NTS) "s_add_u32 m0, m0, 0x400\n\ts_add_u32 %0, %0, %6\n\t"
template <int SEL> DI void dma_tile(__amdgpu_buffer_rsrc_t rs, unsigned voe, unsigned voo, unsigned soff, unsigned p1, unsigned p2, unsigned ldsaddr) {
  const unsigned d2 = p2 - p1;
  if constexpr (SEL == 0)
    asm volatile("s_mov_b32 m0, %1\n\ts_nop 0\n\t" DMA_2("%2", "%3", "") DMA_2("%2", "%3", "") DMA_2("%2", "%3", "") DMA_2("%2", "%3", "") DMA_2("%2", "%3", "") DMA_2("%2", "%3", "") DMA_2("%2", "%3", "") DMA_2("%2", "%3", "")
                 : "+&s"(soff) : "s"(ldsaddr), "v"(voe), "v"(voo), "s"(rs), "s"(p1), "s"(d2) : "memory", "scc");
  else
    asm volatile("s_mov_b32 m0, %1\n\ts_nop 0\n\t" DMA_2("%2", "%2", "nt ") DMA_2("%3", "%3", "nt ") DMA_2("%2", "%2", "nt ") DMA_2("%3", "%3", "nt ") DMA_2("%2", "%2", "nt ") DMA_2("%3", "%3", "nt ") DMA_2("%2", "%2", "nt ") DMA_2("%3", "%3", "nt ")
                 : "+&s"(soff) : "s"(ldsaddr), "v"(voe), "v"(voo), "s"(rs), "s"(p1), "s"(d2) : "memory", "scc");
}
DI bf16x8 rd128(const LAS char* p) { return *(const LAS bf16x8*)p; }
DI bf16x8 rdtr(const LAS char* lo, const LAS char* up) {
  s16x4 a = __builtin_amdgcn_ds_read_tr16_b64_v4i16((LAS s16x4*)lo);
  s16x4 b = __builtin_amdgcn_ds_read_tr16_b64_v4i16((LAS s16x4*)up);
  s16x8 c = __builtin_shufflevector(a, b, 0, 1, 2, 3, 4, 5, 6, 7);
  return __builtin_bit_cast(bf16x8, c);
}
#define BAR() asm volatile("s_barrier" ::: "memory")
#define BAR_L() asm volatile("s_waitcnt lgkmcnt(0)\n\ts_barrier" ::: "memory")
#define WAITV(N) asm volatile("s_waitcnt vmcnt(" #N ")" ::: "memory")
#define WAITV_BAR(N) asm volatile("s_waitcnt vmcnt(" #N ")\n\ts_barrier" ::: "memory")
#define MX3(a, b, c) __builtin_fmaxf(__builtin_fmaxf((a), (b)), (c))
DI float both_max(float x) { auto r = __builtin_amdgcn_permlane32_swap(__float_as_uint(x), __float_as_uint(x), false, false); return __builtin_fmaxf(__uint_as_float(r[0]), __uint_as_float(r[1])); }
DI float both_sum(float x) { auto r = __builtin_amdgcn_permlane32_swap(__float_as_uint(x), __float_as_uint(x), false, false); return __uint_as_float(r[0]) + __uint_as_float(r[1]); }
DI void mfma_acc(f32x16& d, const bf16x8& a, const bf16x8& b) { asm volatile("v_mfma_f32_32x32x16_bf16 %0, %1, %2, %0" : "+v"(d) : "v"(a), "v"(b) : "memory"); }
DI void mfma_new(f32x16& d, const bf16x8& a, const bf16x8& b, const f32x16& c) { asm volatile("v_mfma_f32_32x32x16_bf16 %0, %1, %2, %3" : "=&v"(d) : "v"(a), "v"(b), "v"(c) : "memory"); }
DI float aexp(float x) { asm volatile("v_exp_f32 %0, %0" : "+v"(x) :: "memory"); return x; }
DI unsigned acvt(float a, float b) { unsigned r; asm volatile("v_cvt_pk_bf16_f32 %0, %1, %2" : "=v"(r) : "v"(a), "v"(b) : "memory"); return r; }
DI void aadd1(float& l, float a) { asm volatile("v_add_f32 %0, %0, %1" : "+v"(l) : "v"(a) : "memory"); }
DI void aadd2(float& l, float a, float b) { asm volatile("v_add_f32 %0, %0, %1\n\tv_add_f32 %0, %0, %2" : "+&v"(l) : "v"(a), "v"(b) : "memory"); }
DI void aadd3(float& l, float a, float b, float c) { asm volatile("v_add_f32 %0, %0, %1\n\tv_add_f32 %0, %0, %2\n\tv_add_f32 %0, %0, %3" : "+&v"(l) : "v"(a), "v"(b), "v"(c) : "memory"); }
DI void aadd4(float& l, float a, float b, float c, float d) { asm volatile("v_add_f32 %0, %0, %1\n\tv_add_f32 %0, %0, %2\n\tv_add_f32 %0, %0, %3\n\tv_add_f32 %0, %0, %4" : "+&v"(l) : "v"(a), "v"(b), "v"(c), "v"(d) : "memory"); }
DI float amax7(float a, float b, float c, float d, float e, float f, float g) { float r; asm volatile("v_max3_f32 %0, %1, %2, %3\n\tv_max3_f32 %0, %0, %4, %5\n\tv_max3_f32 %0, %0, %6, %7" : "=&v"(r) : "v"(a), "v"(b), "v"(c), "v"(d), "v"(e), "v"(f), "v"(g) : "memory"); return r; }
DI void amax6(float& r, float a, float b, float c, float d, float e, float f) { asm volatile("v_max3_f32 %0, %0, %1, %2\n\tv_max3_f32 %0, %0, %3, %4\n\tv_max3_f32 %0, %0, %5, %6" : "+&v"(r) : "v"(a), "v"(b), "v"(c), "v"(d), "v"(e), "v"(f) : "memory"); }
DI void amax8(float& r, float a, float b, float c, float d, float e, float f, float g, float h) { asm volatile("v_max3_f32 %0, %0, %1, %2\n\tv_max3_f32 %0, %0, %3, %4\n\tv_max3_f32 %0, %0, %5, %6\n\tv_max3_f32 %0, %0, %7, %8" : "+&v"(r) : "v"(a), "v"(b), "v"(c), "v"(d), "v"(e), "v"(f), "v"(g), "v"(h) : "memory"); }
DI void amax3z(float& r, float a, float b, float c) { asm volatile("v_max3_f32 %0, %0, %1, %2\n\tv_max3_f32 %0, %0, %3, 0" : "+&v"(r) : "v"(a), "v"(b), "v"(c) : "memory"); }
DI int nxt5(int s) { return s == 4 * SLOT ? 0 : s + SLOT; }
struct Tile { u32x4 v[16]; };
DI void tile_init(Tile& t) {
#pragma unroll
  for (int p = 0; p < 16; ++p) asm volatile("" : "=v"(t.v[p]));
}
template <int AUX, int IMM> DI void ld16(u32x4& d, __amdgpu_buffer_rsrc_t rs, unsigned voff, unsigned soff) {
  if constexpr (AUX == NT) asm volatile("buffer_load_dwordx4 %0, %1, %2, %3 offen offset:%4 nt" : "+v"(d) : "v"(voff), "s"(rs), "s"(soff), "i"(IMM) : "memory");
  else                     asm volatile("buffer_load_dwordx4 %0, %1, %2, %3 offen offset:%4" : "+v"(d) : "v"(voff), "s"(rs), "s"(soff), "i"(IMM) : "memory");
}
template <int P> DI void wr16(const u32x4& d, unsigned addr) { asm volatile("ds_write_b128 %0, %1 offset:%2" ::"v"(addr), "v"(d), "i"(P * 1024) : "memory"); }
DI void tile_wr(const Tile& t, unsigned addr) {
  wr16<0>(t.v[0], addr); wr16<1>(t.v[1], addr); wr16<2>(t.v[2], addr); wr16<3>(t.v[3], addr); wr16<4>(t.v[4], addr); wr16<5>(t.v[5], addr); wr16<6>(t.v[6], addr); wr16<7>(t.v[7], addr);
  wr16<8>(t.v[8], addr); wr16<9>(t.v[9], addr); wr16<10>(t.v[10], addr); wr16<11>(t.v[11], addr); wr16<12>(t.v[12], addr); wr16<13>(t.v[13], addr); wr16<14>(t.v[14], addr); wr16<15>(t.v[15], addr);
}

__global__ __launch_bounds__(512, 2) void mha_fused(const char* x, const char* kc, const char* vc, const char* wq, const char* wo, char* out, char* ws) {
  __shared__ __attribute__((aligned(16))) char lds[LDS_BYTES];
  u64 pc0; asm volatile("s_getpc_b64 %0" : "=s"(pc0));
  const int tid = threadIdx.x, lane = tid & 63;
  const int wid = rfl(tid >> 6);
  const int wg = blockIdx.x;
  const int xcd = wg & 7, j = wg >> 3;
  const int b = j & 7, h = xcd * 4 + (j >> 3);
  const bool late = (j ^ (j >> 3)) & 1;
  const int kq = xcd >> 1, ob = (xcd & 1) * 4 + (j >> 3), nb = j & 7;
  auto hmap = [&](int p) { return 8 * kq + 2 * (p & 3) + ((ob & 1) ^ (p >> 2)); };
  const unsigned lds0 = (unsigned)(size_t)(LAS char*)lds;

  const __amdgpu_buffer_rsrc_t rcode = SRD(pc0 & ~255ull, CODE_KB * 1024);
  u32x4 cw; asm volatile("" : "=v"(cw));
  if (wid < 4) ld16<0, 0>(cw, rcode, lane * 16u, ((j & 7) * 4 + wid) * 1024u);
  asm volatile("" : "+s"(x), "+s"(kc), "+s"(vc), "+s"(wq), "+s"(wo), "+s"(out), "+s"(ws));
  GAS u64* flags = (GAS u64*)(ws + WS_FLAGS);

  if (__builtin_expect(wid >= 4, 1)) {
    const unsigned r8 = (lane >> 2) & 7, sub = lane >> 5, c4 = lane & 3;
    const unsigned hb = wid & 1;
    const unsigned xe = hb ? c4 : (c4 ^ (r8 >> 2)), xo = hb ? c4 : (c4 ^ (2 + (r8 >> 2)));
    const unsigned ldh = lds0 + hb * 16384u;
#define KV_SETUP() const __amdgpu_buffer_rsrc_t rkv = SRD((hb ? vc : kc) + (size_t)(b * NH + h) * SK * DH * 2, SK * DH * 2); \
    const unsigned kve = r8 * 256u + sub * 64u + 16u * xe, kvo = r8 * 256u + sub * 64u + 16u * xo; \
    const int krot = (j + 5 * xcd) & 15;     \
    const unsigned wdst = ldh + lane * 16u; \
    auto tile_ld4 = [&](Tile& R, int t, int q) { const unsigned so = ((t + krot) & 15) * 16384u + q * 4096u; \
      ld16<NT, 0>(R.v[4 * q], rkv, kve, so); ld16<NT, 128>(R.v[4 * q + 1], rkv, kve, so); ld16<NT, 2048>(R.v[4 * q + 2], rkv, kvo, so); ld16<NT, 2048 + 128>(R.v[4 * q + 3], rkv, kvo, so); }
    if (__builtin_expect(wid >= 6, 0)) {
      KV_SETUP();
      auto tile_ld = [&](Tile& R, int t) { tile_ld4(R, t, 0); BWAIT(); tile_ld4(R, t, 1); BWAIT(); tile_ld4(R, t, 2); BWAIT(); tile_ld4(R, t, 3); BWAIT(); };
      auto tile_dma = [&](int t, int so) { dma_tile<1>(rkv, kve, kvo, ((t + krot) & 15) * 16384u, 128u, 2048u, ldh + so); };
      const __amdgpu_buffer_rsrc_t rwo = SRD(wo + nb * 256, NH * DH * DM * 2 - nb * 256);
      const unsigned vw0 = (hb * 64u + r8) * 2048u + 16u * c4 + 64u * ((kq + sub) & 3), vw1 = (hb * 64u + r8) * 2048u + 16u * c4 + 64u * ((kq + 2 + sub) & 3);
      auto w_dma = [&](int p, int so) { dma_tile<0>(rwo, vw0, vw1, (unsigned)hmap(p) * (128u * 2048u), 0u, 16384u, ldh + so); };
      int sl = 3 * SLOT;
      int n0 = 5;
      if (late) {
        Tile R0, R1, R2;
        tile_init(R0); tile_init(R1); tile_init(R2);
#pragma unroll 1
        for (int c = -1; c <= KV_B; ++c) BAR();
        tile_dma(0, 3 * SLOT); BWAIT(); tile_dma(1, 4 * SLOT); BWAIT(); tile_ld(R0, 2);
        WAITV(32); tile_ld(R1, 3);
        WAITV(32); tile_ld(R2, 4);
#pragma unroll 1
        for (int c = KV_B + 1; c <= 14; ++c) BAR();
        WAITV_BAR(0);
        tile_wr(R0, wdst);
        BAR_L(); tile_wr(R1, wdst + SLOT);
        if (hb) BAR_L();
        tile_wr(R2, wdst + 2 * SLOT);
        asm volatile("s_waitcnt lgkmcnt(0)" ::: "memory");
      } else {
#pragma unroll 1
        for (int c = -1; c <= 12 + (int)hb; ++c) BAR();
        __builtin_amdgcn_s_sleep(6);
        tile_dma(0, sl); sl = nxt5(sl);
        n0 = 1;
      }
#pragma unroll 1
      for (int n = n0; n <= ((XA && !late) ? 12 : 15); ++n) { WAITV_BAR(32); tile_dma(n, sl); sl = nxt5(sl); }
      if (XA && !late) { WAITV_BAR(32); WAITV_BAR(16); WAITV_BAR(0); sl = 4 * SLOT; }
      if (!hb) {
        WAITV_BAR(32); WAITV_BAR(16); w_dma(0, sl); sl = nxt5(sl);
        WAITV_BAR(16); w_dma(1, sl); sl = nxt5(sl);
        BAR(); w_dma(2, sl); sl = nxt5(sl);
      } else {
#pragma unroll 1
        for (int s = 0; s <= 2; ++s) { WAITV_BAR(32); w_dma(s, sl); sl = nxt5(sl); }
      }
      WAITV_BAR(32);
      WAITV(31);
      __builtin_amdgcn_s_sleep(8);
      w_dma(3, sl); sl = nxt5(sl); w_dma(4, sl);
#pragma unroll 1
      for (int p = 5; p < 8; ++p) { BAR(); WAITV_BAR(47); sl = nxt5(sl); w_dma(p, sl); }
      BAR(); WAITV_BAR(47); BAR(); WAITV_BAR(32); BAR(); WAITV_BAR(16); BAR(); WAITV_BAR(0); BAR(); BAR();
      return;
    }
    u64 tag = 0;
    {
      const int qrot = (2 * (j & 7) + 4 * (j >> 3)) & 15;
      u64 qbase = hb ? (u64)wq : (u64)x; unsigned qoff = hb ? h * DH * 2 : b * SQ * DM * 2, qn = hb ? DM * NH * DH * 2 : (b + 1) * SQ * DM * 2;
      unsigned rsh = hb ? 13u : 11u, cstr = hb ? 64u * 8192u : 128u, p1 = hb ? 128u : 16384u, p2 = hb ? 65536u : 32768u;
      asm volatile("" : "+s"(qbase), "+s"(qoff), "+s"(qn), "+s"(rsh), "+s"(cstr), "+s"(p1), "+s"(p2));
      const __amdgpu_buffer_rsrc_t rs = SRD(qbase, qn);
      const unsigned voe = (r8 << rsh) + sub * 64u + 16u * xe, voo = (r8 << rsh) + sub * 64u + 16u * xo;
      int sl = 2 * SLOT;
      tag = __hip_atomic_load((GAS u64*)(ws + WS_EPOCH), RLX_AGENT);
#pragma unroll 1
      for (int c = 0; c < 16; ++c) {
        dma_tile<0>(rs, voe, voo, qoff + ((c + qrot) & 15) * cstr, p1, p2, ldh + sl); sl = sl == 2 * SLOT ? 0 : sl + SLOT;
        if (c == 2) { WAITV_BAR(32); LS(); }
        if (c >= 2) { WAITV_BAR(16); LS(); }
      }
      WAITV_BAR(0); LS(); BAR(); LS();
    }
    tag += EPOCH_INC;
    {
      u64 seen = 0;
      const GAS u64* myflag = flags + (((ob * 4 + kq) * 8 + nb) * 32 + (lane & 31));
      auto gate = [&](u64 need) {
        if (hb) return;
#pragma unroll 1
        for (unsigned spins = 0; (seen & need) != need && spins < SPIN_CAP; ++spins) {
          seen = __ballot(__hip_atomic_load(myflag, RLX_AGENT) == tag);
          if ((seen & need) != need) __builtin_amdgcn_s_sleep(1);
        }
      };
      auto hbit = [&](int p) { return 0x01010101ull << (hmap(p) & 7); };
      if (XA && !late) {
        KV_SETUP();
        Tile R0, R1, R2;
        tile_init(R0); tile_init(R1); tile_init(R2);
#pragma unroll 1
        for (int t = 0; t < XA0; ++t) { BAR(); LS(); }
        BAR(); LS(); tile_ld4(R0, 13, 0); tile_ld4(R0, 13, 1);
        BAR(); LS(); tile_ld4(R0, 13, 2); tile_ld4(R0, 13, 3);
        BAR(); LS(); tile_ld4(R1, 14, 0); tile_ld4(R1, 14, 1);
        BAR(); LS(); tile_ld4(R1, 14, 2); tile_ld4(R1, 14, 3);
        BAR(); LS(); tile_ld4(R2, 15, 0); tile_ld4(R2, 15, 1);
        BAR(); LS(); tile_ld4(R2, 15, 2); tile_ld4(R2, 15, 3);
#pragma unroll 1
        for (int t = XA0 + 6; t <= 9; ++t) { BAR(); LS(); }
        WAITV_BAR(0); LS(); tile_wr(R0, wdst + SLOT);
        BAR_L(); LS(); tile_wr(R1, wdst + 2 * SLOT);
        ld16<0, 0>(R0.v[0], rcode, lane * 16u, ((j >> 1) * 2 + hb) * 1024u);
        BAR_L(); LS(); tile_wr(R2, wdst + 3 * SLOT);
        BAR_L(); LS(); WAITV_BAR(0); LS();
        asm volatile("" ::"v"(R0.v[0]));
      } else {
#pragma unroll 1
        for (int t = 0; t <= 10; ++t) { BAR(); LS(); }
        u32x4 d0; asm volatile("" : "=v"(d0));
        ld16<0, 0>(d0, rcode, lane * 16u, ((j >> 1) * 2 + hb) * 1024u);
        BAR(); LS(); BAR(); LS(); BAR(); LS(); WAITV_BAR(0); LS();
        asm volatile("" ::"v"(d0));
      }
      BAR(); LS();
      gate(hbit(0) | hbit(1)); BAR(); LS();
#pragma unroll 1
      for (int m = 0; m < 8; ++m) { if (m < 6) gate(hbit(m + 2)); BAR(); BAR(); LS(); }
    }
    if (wg == 0 && wid == 4) {
#pragma unroll 1
      for (unsigned spins = 0; spins < SPIN_CAP; ++spins) {
        bool ok = true;
#pragma unroll
        for (int i = 0; i < 16; ++i) ok &= __hip_atomic_load(flags + ((2 * i + (lane >> 5)) * 256 + (lane & 31)), RLX_AGENT) == tag;
        if (__all(ok)) break;
        __builtin_amdgcn_s_sleep(2);
      }
      if (lane == 0) __hip_atomic_store((GAS u64*)(ws + WS_EPOCH), tag, RLX_AGENT);
    }
    return;
  }
  STAMP(0);
  const u64 tag = __hip_atomic_load((GAS u64*)(ws + WS_EPOCH), RLX_AGENT) + EPOCH_INC;
  const int r = lane & 31, hh = lane >> 5, qb = wid;
  const int tq = (lane >> 2) & 3, tp = lane & 3, blk = (lane >> 4) & 1;
  const LAS char* L = (const LAS char*)lds;
  const int rowe = 64 * (r & 7) + 16 * (hh ^ ((r >> 2) & 3));
  const int kbase_e = 2048 * (r >> 3) + rowe, kbase_o = kbase_e ^ 32;
  const int tbase = hh * 256 + tq * 64 + blk * 32 + tp * 8;
  bf16x8 qf[8];
  bf16x8 kf[16], vf[16];
  f32x16 acc[4] = {};
#define KRD(i, SP) kf[i] = rd128((SP) + ((((i) >> 1) & 1) ? kbase_o : kbase_e) + 512 * ((i) >> 2) + 8192 * ((i) & 1))
  {
    const int xbase_e = 1024 * (4 * qb + (r >> 3)) + rowe, xbase_o = xbase_e ^ 32;
    const int pp = (tp == 1) ? 2 : (tp == 2) ? 1 : tp;
    const int wbase = 16384 + hh * 2048 + tq * 64 + blk * 32 + pp * 8;
    bf16x8 xf[4], wf[16];
    int sl = 2 * SLOT;
#define WRD(i, SP) wf[i] = rdtr((SP) + wbase + 4096 * ((i) >> 2) + 512 * ((i) & 3), (SP) + wbase + 4096 * ((i) >> 2) + 512 * ((i) & 3) + 256)
#define XRD(ks, SP) xf[ks] = rd128((SP) + (((ks) & 1) ? xbase_o : xbase_e) + 512 * ((ks) >> 1))
#define GQ(g, BODY) do { acc[(g) & 3] = MFMA(wf[g], xf[(g) >> 2], acc[(g) & 3]); BODY; SBAR(); } while (0)
    __builtin_amdgcn_s_waitcnt(0xC07F);
    asm volatile("s_waitcnt vmcnt(0)\n\ts_barrier" ::"v"(cw) : "memory");
    STAMP(1);
    { const LAS char* S = L + sl; XRD(0, S); WRD(0, S); SBAR(); WRD(1, S); WRD(2, S); SBAR(); WRD(3, S); XRD(1, S); SBAR(); WRD(4, S); SBAR(); WRD(5, S); SBAR(); }
#define QCHUNK(LAST) do { const LAS char* S = L + sl; sl = sl == 2 * SLOT ? 0 : sl + SLOT; const LAS char* N = (LAST) ? L + 3 * SLOT : L + sl; SBAR(); \
      GQ(0, { WRD(6, S); XRD(2, S); }); GQ(1, { WRD(7, S); XRD(3, S); }); GQ(2, { WRD(8, S); }); GQ(3, { WRD(9, S); WRD(10, S); }); GQ(4, WRD(11, S)); GQ(5, { WRD(12, S); WRD(13, S); }); \
      GQ(6, { WRD(14, S); }); GQ(7, WRD(15, S)); GQ(8, {}); GQ(9, {}); GQ(10, {}); \
      BAR_L(); SBAR(); GQ(11, {}); \
      if (!(LAST)) { XRD(0, N); WRD(0, N); SBAR(); GQ(12, { WRD(1, N); WRD(2, N); }); GQ(13, { WRD(3, N); XRD(1, N); }); GQ(14, { WRD(4, N); }); GQ(15, WRD(5, N)); } \
      else { GQ(12, { KRD(0, N); KRD(1, N); }); GQ(13, { KRD(2, N); KRD(3, N); }); GQ(14, { KRD(4, N); KRD(5, N); }); GQ(15, { KRD(6, N); KRD(7, N); }); } } while (0)
#pragma unroll 1
    for (int c = 0; c < 15; ++c) QCHUNK(false);
    QCHUNK(true);
#pragma unroll
    for (int d = 0; d < 4; ++d)
#pragma unroll
      for (int i = 0; i < 16; ++i) acc[d][i] *= C2;
#pragma unroll
    for (int s = 0; s < 8; ++s) qf[s] = pack8(acc[s >> 1], s & 1);
  }
  STAMP(2);
  {
    f32x16 o[4] = {};
    float l = 0.f;
    f32x16 sA0, sA1, sB0, sB1, negm;
    u32x4 pw[4];
    const f32x16 zero16 = {};
    int sk = 3 * SLOT;
    int sv = 3 * SLOT + 16384;
#define VRD(i, SP) vf[i] = rdtr((SP) + tbase + 4096 * ((i) >> 2) + 512 * ((i) & 3), (SP) + tbase + 4096 * ((i) >> 2) + 512 * ((i) & 3) + 2048)
#define EX(v) __builtin_amdgcn_exp2f(v)
#define PF(k) __builtin_bit_cast(bf16x8, pw[k])
#define LW(n) __builtin_amdgcn_s_waitcnt(0xC07F | ((n) << 8))
    {
      const LAS char* KP = L + sk;
#pragma unroll
      for (int i = 8; i < 16; ++i) KRD(i, KP);
      sA0 = MFMA(kf[0], qf[0], zero16); sA1 = MFMA(kf[1], qf[0], zero16);
#pragma unroll
      for (int s = 1; s < 8; ++s) { sA0 = MFMA(kf[2 * s], qf[s], sA0); sA1 = MFMA(kf[2 * s + 1], qf[s], sA1); }
      BAR_L();
      STAMP(3);
      sk = nxt5(sk);
      const LAS char* KN = L + sk;
      KRD(0, KN); SBAR(); KRD(1, KN); SBAR(); KRD(2, KN); SBAR(); KRD(3, KN); SBAR();
      float a = MX3(sA0[0], sA0[1], sA1[0]); a = MX3(a, sA1[1], sA0[2]);
#pragma unroll
      for (int i = 3; i < 16; i += 2) a = MX3(a, sA0[i], (i + 1 < 16) ? sA0[i + 1] : sA0[i]);
#pragma unroll
      for (int i = 2; i < 16; i += 2) a = MX3(a, sA1[i], sA1[i + 1]);
      const float m = both_max(a);
#pragma unroll
      for (int i = 0; i < 16; ++i) { sA0[i] -= m; sA1[i] -= m; negm[i] = -m; }
#pragma unroll
      for (int i = 0; i < 13; ++i) sA0[i] = EX(sA0[i]);
    }
#define GA(i, X, BODY) do { if ((i) < 2) mfma_new(X, kf[i], qf[0], negm); else mfma_acc(X, kf[i], qf[(i) >> 1]); if ((i) + 4 < 16) { KRD(((i) + 4) & 15, KP_); } else { VRD((i) & 3, VP_); } BODY; } while (0)
#define E1(Y, e) do { Y[e] = aexp(Y[e]); } while (0)
#define E2(Y, e) do { Y[e] = aexp(Y[e]); Y[e + 1] = aexp(Y[e + 1]); } while (0)
#define S1(Y, e) aadd1(l, Y[e])
#define S2(Y, e) aadd2(l, Y[e], Y[e + 1])
#define S3(Y, e) aadd3(l, Y[e], Y[e + 1], Y[e + 2])
#define S4(Y, e) aadd4(l, Y[e], Y[e + 1], Y[e + 2], Y[e + 3])
#define PK(k, d, Y, e) do { pw[k][d] = acvt(Y[e], Y[e + 1]); } while (0)
#define PHASE_A(X0, X1, Y0, Y1) do { const LAS char* KP_ = L + sk; const LAS char* VP_ = L + sv; \
      LW(2); GA(0,  X0, { E2(Y0, 13); }); \
             GA(1,  X1, { PK(0, 0, Y0, 0); E1(Y0, 15); E1(Y1, 0); }); \
      LW(2); GA(2,  X0, { E2(Y1, 1); }); \
             GA(3,  X1, { PK(0, 1, Y0, 2); E2(Y1, 3); }); \
      LW(2); GA(4,  X0, { E2(Y1, 5); asm volatile("" ::"v"(negm));   }); \
             GA(5,  X1, { PK(0, 2, Y0, 4); E2(Y1, 7); }); \
      LW(2); GA(6,  X0, { E2(Y1, 9); }); \
             GA(7,  X1, { PK(0, 3, Y0, 6); E2(Y1, 11); }); \
      LW(2); GA(8,  X0, { E2(Y1, 13); }); \
             GA(9,  X1, { PK(1, 0, Y0, 8); S2(Y0, 0); E1(Y1, 15); }); \
      LW(2); GA(10, X0, { PK(1, 1, Y0, 10); S3(Y0, 2); }); \
             GA(11, X1, { PK(1, 2, Y0, 12); S4(Y0, 5); }); \
      LW(2); GA(12, X0, { PK(1, 3, Y0, 14); S2(Y0, 9); }); \
             GA(13, X1, { S4(Y0, 11); }); \
      LW(4); GA(14, X0, { aadd3(l, Y0[15], Y1[0], Y1[1]); }); \
             GA(15, X1, { S2(Y1, 2); }); \
      } while (0)
#define GB(i, NEXTK, BODY) do { mfma_acc(o[(i) & 3], vf[i], PF((i) >> 2)); if ((i) + 4 < 16) { VRD(((i) + 4) & 15, VP_); } else if (NEXTK) { KRD((i) & 3, KN_); } BODY; } while (0)
#define PHASE_B(X0, X1, Y1, NEXTK) do { const LAS char* VP_ = L + sv; const LAS char* KN_ = L + sk; float a_; float alpha_ = 1.f; bool resc_ = false; \
      LW(4); GB(0, NEXTK, { PK(2, 0, Y1, 0); S2(Y1, 4); }); \
             GB(1, NEXTK, { PK(2, 1, Y1, 2); PK(2, 2, Y1, 4); PK(2, 3, Y1, 6); S1(Y1, 6); }); \
      LW(4); GB(2, NEXTK, { a_ = amax7(X0[0], X0[1], X0[2], X0[3], X0[4], X0[5], X0[6]); }); \
             GB(3, NEXTK, { amax8(a_, X0[7], X0[8], X0[9], X0[10], X0[11], X0[12], X0[13], X0[14]); }); \
      LW(4); GB(4, NEXTK, { amax6(a_, X0[15], X1[0], X1[1], X1[2], X1[3], X1[4]); }); \
             GB(5, NEXTK, { amax8(a_, X1[5], X1[6], X1[7], X1[8], X1[9], X1[10], X1[11], X1[12]); }); \
      LW(4); GB(6, NEXTK, { amax3z(a_, X1[13], X1[14], X1[15]); S1(Y1, 7); }); \
             GB(7, NEXTK, { S2(Y1, 8); }); \
      if (__builtin_expect(__any(a_ > THR), 0)) { const float dl_ = both_max(a_); alpha_ = __builtin_amdgcn_exp2f(-dl_); resc_ = true; \
        _Pragma("unroll") for (int i_ = 0; i_ < 16; ++i_) { X0[i_] -= dl_; X1[i_] -= dl_; negm[i_] -= dl_; } } \
      LW(4); GB(8,  NEXTK, { PK(3, 0, Y1, 8); PK(3, 1, Y1, 10); S1(Y1, 10); }); \
             GB(9,  NEXTK, { PK(3, 2, Y1, 12); PK(3, 3, Y1, 14); E1(X0, 0); }); \
      LW(4); GB(10, NEXTK, { S1(Y1, 11); E1(X0, 1); }); \
             GB(11, NEXTK, { E2(X0, 2); }); \
      LW(4); GB(12, NEXTK, { S1(Y1, 12); E2(X0, 4); }); \
             GB(13, NEXTK, { S1(Y1, 13); E2(X0, 6); }); \
      if (NEXTK) LW(2); else LW(0); \
             GB(14, NEXTK, { S1(Y1, 14); E2(X0, 8); }); \
             GB(15, NEXTK, { S1(Y1, 15); E2(X0, 10); E1(X0, 12); }); \
      if (__builtin_expect(resc_, 0)) { asm volatile("s_nop 7\n\ts_nop 7" : "+v"(o[0]), "+v"(o[1]), "+v"(o[2]), "+v"(o[3]) :: "memory"); \
        l *= alpha_; _Pragma("unroll") for (int d_ = 0; d_ < 4; ++d_) _Pragma("unroll") for (int i_ = 0; i_ < 16; ++i_) o[d_][i_] *= alpha_; } \
      sv = sv == 4 * SLOT + 16384 ? 16384 : sv + SLOT; } while (0)
    asm volatile("s_nop 1" : "+v"(negm), "+v"(sA0), "+v"(sA1) :: "memory");
#pragma unroll 1
    for (int t = 1; t < 15; t += 2) {
      PHASE_A(sB0, sB1, sA0, sA1); BAR(); sk = nxt5(sk); PHASE_B(sB0, sB1, sA1, true);
      PHASE_A(sA0, sA1, sB0, sB1); BAR(); sk = nxt5(sk); PHASE_B(sA0, sA1, sB1, true);
    }
    PHASE_A(sB0, sB1, sA0, sA1); BAR(); PHASE_B(sB0, sB1, sA1, false);
    {
      const LAS char* VP_ = L + sv;
#pragma unroll
      for (int i = 0; i < 16; ++i) VRD(i, VP_);
#pragma unroll
      for (int i = 13; i < 16; ++i) sB0[i] = EX(sB0[i]);
      const bf16x8 p0 = pack8(sB0, 0), p1 = pack8(sB0, 1);
#pragma unroll
      for (int db = 0; db < 4; ++db) o[db] = MFMA(vf[db], p0, o[db]);
#pragma unroll
      for (int i = 0; i < 16; ++i) sB1[i] = EX(sB1[i]);
#pragma unroll
      for (int db = 0; db < 4; ++db) o[db] = MFMA(vf[4 + db], p1, o[db]);
      const bf16x8 p2 = pack8(sB1, 0), p3 = pack8(sB1, 1);
#pragma unroll
      for (int db = 0; db < 4; ++db) o[db] = MFMA(vf[8 + db], p2, o[db]);
#pragma unroll
      for (int i = 0; i < 16; ++i) l += sB0[i];
#pragma unroll
      for (int db = 0; db < 4; ++db) o[db] = MFMA(vf[12 + db], p3, o[db]);
#pragma unroll
      for (int i = 0; i < 16; ++i) l += sB1[i];
      l = both_sum(l);
    }
    STAMP(7);
    const float inv = __builtin_amdgcn_rcpf(l);
    const __amdgpu_buffer_rsrc_t rs = SRD(ws + (unsigned)(((b * 4 + qb) * 256 + h * 8) * 1024), 8192);
#pragma unroll
    for (int db = 0; db < 4; ++db) {
#pragma unroll
      for (int i = 0; i < 16; ++i) o[db][i] *= inv;
#pragma unroll
      for (int s = 0; s < 2; ++s) __builtin_amdgcn_raw_buffer_store_b128(__builtin_bit_cast(u32x4, pack8(o[db], s)), rs, lane * 16 + (db * 2 + s) * 1024, 0, SC1);
    }
#pragma unroll
    for (int i = 0; i < 4; ++i) acc[i] = (f32x16){};
    asm volatile("s_waitcnt vmcnt(0)" : "+v"(acc[0]), "+v"(acc[1]), "+v"(acc[2]), "+v"(acc[3]) :: "memory");
    if (lane < 8) __hip_atomic_store(flags + (((b * 4 + (h >> 3)) * 8 + lane) * 32 + qb * 8 + (h & 7)), tag, RLX_AGENT);
  }
  STAMP(8);
  {
    BAR_L();
    STAMP(9);
    const __amdgpu_buffer_rsrc_t rso = SRD(ws + (size_t)(ob * 4 + qb) * 262144, 262144);
    u32x4 bq[2][8];
    bf16x8 wf[16];
    int sw = 4 * SLOT;
#define OL1(lb, lh, so_, i_) bq[lb][4 * (lh) + (i_)] = __builtin_amdgcn_raw_buffer_load_b128(rso, lane * 16 + (i_) * 1024, so_, SC1)
#define OLD4(buf, p, hf) do { const unsigned so_ = (unsigned)hmap(p) * 8192u + (hf) * 4096u; _Pragma("unroll") for (int i_ = 0; i_ < 4; ++i_) { OL1(buf, hf, so_, i_); SBAR(); } } while (0)
#define ORD(i, SP) wf[i] = rdtr((SP) + tbase + 4096 * ((i) >> 2) + 512 * ((i) & 3), (SP) + tbase + 4096 * ((i) >> 2) + 512 * ((i) & 3) + 2048)
#define GO(buf, hf, g, BODY) do { acc[(g) & 3] = MFMA(wf[g], __builtin_bit_cast(bf16x8, bq[buf][4 * (hf) + ((g) >> 2)]), acc[(g) & 3]); BODY; SBAR(); } while (0)
#define OHALF(buf, hf, S, N, SYNC, MORE, LD, lb, lh, lp) do { unsigned so_ = 0; SBAR(); \
      GO(buf, hf, 0, { ORD(6, S); so_ = (unsigned)hmap(lp) * 8192u + (lh) * 4096u + (unsigned)((lp) >> 3) * 0x40000000u; asm volatile("" : "+s"(so_)); }); GO(buf, hf, 1, { ORD(7, S); if (LD) OL1(lb, lh, so_, 0); }); \
      GO(buf, hf, 2, { ORD(8, S); if (LD) OL1(lb, lh, so_, 1); }); GO(buf, hf, 3, { ORD(9, S); if (LD) OL1(lb, lh, so_, 2); }); GO(buf, hf, 4, { ORD(10, S); if (LD) OL1(lb, lh, so_, 3); }); \
      GO(buf, hf, 5, { ORD(11, S); ORD(12, S); }); GO(buf, hf, 6, { ORD(13, S); ORD(14, S); }); GO(buf, hf, 7, ORD(15, S)); GO(buf, hf, 8, {}); GO(buf, hf, 9, {}); GO(buf, hf, 10, {}); \
      if ((SYNC) == 1) { BAR_L(); SBAR(); } else if ((SYNC) == 2) { BAR(); SBAR(); } \
      GO(buf, hf, 11, if (MORE) ORD(0, N)); GO(buf, hf, 12, if (MORE) { ORD(1, N); ORD(2, N); }); GO(buf, hf, 13, if (MORE) ORD(3, N)); GO(buf, hf, 14, if (MORE) ORD(4, N)); GO(buf, hf, 15, if (MORE) ORD(5, N)); } while (0)
#define OSTAGE(buf, p, LAST, LD2) do { const LAS char* S_ = L + sw; sw = nxt5(sw); const LAS char* N_ = L + sw; \
      OHALF(buf, 0, S_, S_ + 16384, (LAST) ? 0 : 2, true, !(LAST), (buf) ^ 1, 1, (p) + 1); \
      OHALF(buf, 1, S_ + 16384, N_, (LAST) ? 0 : 1, !(LAST), LD2, buf, 0, (p) + 2); } while (0)
    OLD4(0, 0, 0); OLD4(0, 0, 1); OLD4(1, 1, 0);
    { const LAS char* S_ = L + sw; ORD(0, S_); SBAR(); ORD(1, S_); ORD(2, S_); SBAR(); ORD(3, S_); SBAR(); ORD(4, S_); SBAR(); ORD(5, S_); SBAR(); }
#pragma unroll 1
    for (int p = 0; p < 8; p += 2) { OSTAGE(0, p, false, true); OSTAGE(1, p + 1, false, true); }
    STAMP(10);
    const int tile = ob * 8 + nb;
    GAS u64* flags2 = (GAS u64*)(ws + WS_FLAGS2) + (tile * 16 + qb) * 16;
    {
      const __amdgpu_buffer_rsrc_t rp = SRD(ws + WS_PART + (size_t)tile * 131072 + (kq * 4 + qb) * 2048, 131072);
#pragma unroll
      for (int i = 1; i < 4; ++i)
#pragma unroll
        for (int g = 0; g < 2; ++g) __builtin_amdgcn_raw_buffer_store_b128(__builtin_bit_cast(u32x4, pack8(acc[i], g)), rp, lane * 16 + ((kq + i) & 3) * 32768u + g * 1024, 0, SC1);
      WAITV(0);
      if (lane < 4) __hip_atomic_store(flags2 + lane * 64 + kq, tag, RLX_AGENT);
    }
    STAMP(4);
#pragma unroll 1
    for (unsigned spins = 0; spins < SPIN_CAP; ++spins) {
      if (__all(__hip_atomic_load(flags2 + kq * 64 + (lane & 3), RLX_AGENT) == tag || (lane & 3) == kq)) break;
      __builtin_amdgcn_s_sleep(1);
    }
    __builtin_amdgcn_fence(__ATOMIC_ACQUIRE, "wavefront");
    STAMP(5);
    {
      const __amdgpu_buffer_rsrc_t rp = SRD(ws + WS_PART + (size_t)tile * 131072 + kq * 32768 + qb * 2048, 32768);
      u32x4 pv[3][2];
#pragma unroll
      for (int i = 1; i < 4; ++i)
#pragma unroll
        for (int g = 0; g < 2; ++g) pv[i - 1][g] = __builtin_amdgcn_raw_buffer_load_b128(rp, lane * 16 + g * 1024, ((kq + i) & 3) * 8192u, SC1);
      f32x16 sum = acc[0];
#pragma unroll
      for (int i = 0; i < 3; ++i)
#pragma unroll
        for (int e = 0; e < 8; ++e) { const unsigned w = pv[i][e >> 2][e & 3]; sum[2 * e] += __uint_as_float(w << 16); sum[2 * e + 1] += __uint_as_float(w & 0xffff0000u); }
      GAS char* orow = (GAS char*)out + (size_t)(ob * 128 + qb * 32 + r) * (DM * 2) + (nb * 128 + kq * 32) * 2 + hh * 16;
#pragma unroll
      for (int g = 0; g < 2; ++g) {
        unsigned a0 = cvtpk(sum[8 * g], sum[8 * g + 1]), a1 = cvtpk(sum[8 * g + 2], sum[8 * g + 3]);
        unsigned b0 = cvtpk(sum[8 * g + 4], sum[8 * g + 5]), b1 = cvtpk(sum[8 * g + 6], sum[8 * g + 7]);
        auto r0 = __builtin_amdgcn_permlane32_swap(a0, b0, false, false);
        auto r1 = __builtin_amdgcn_permlane32_swap(a1, b1, false, false);
        u32x4 w = {r0[0], r1[0], r0[1], r1[1]};
        *(GAS u32x4*)(orow + g * 32) = w;
      }
    }
    STAMP(11);
  }
}

extern "C" void kernel_launch(void* const* d_in, const int* in_sizes, int n_in, void* d_out, int out_size, void* d_ws, size_t ws_size, hipStream_t stream) {
  mha_fused<<<256, 512, 0, stream>>>((const char*)d_in[0], (const char*)d_in[1], (const char*)d_in[2], (const char*)d_in[3], (const char*)d_in[4], (char*)d_out, (char*)d_ws);
}
```

```cpp
#include <hip/hip_runtime.h>
#include <hip/hip_bf16.h>

#define KV_B 2
#define CODE_KB 22

typedef __bf16 bf16x8 __attribute__((ext_vector_type(8)));
typedef __bf16 bf16x2 __attribute__((ext_vector_type(2)));
typedef short s16x4 __attribute__((ext_vector_type(4)));
typedef short s16x8 __attribute__((ext_vector_type(8)));
typedef float f32x16 __attribute__((ext_vector_type(16)));
typedef float f32x4 __attribute__((ext_vector_type(4)));
typedef float f32x2 __attribute__((ext_vector_type(2)));
typedef unsigned u32x4 __attribute__((ext_vector_type(4)));
typedef unsigned long long u64;
#define LAS __attribute__((address_space(3)))
#define GAS __attribute__((address_space(1)))
#define DI __device__ __forceinline__
#define MFMA(a, b, c) __builtin_amdgcn_mfma_f32_32x32x16_bf16((a), (b), (c), 0, 0, 0)
#define SBAR() __builtin_amdgcn_sched_barrier(0)
#define RLX_AGENT __ATOMIC_RELAXED, __HIP_MEMORY_SCOPE_AGENT
#define SRD(p, n) __builtin_amdgcn_make_buffer_rsrc((void*)(p), 0, (n), 0x00020000)

constexpr int SQ = 128, DM = 1024, NH = 32, SK = 1024, DH = 128;
constexpr int SLOT = 32768, LDS_BYTES = 163840;
constexpr float C2 = 0.08838834764831845f * 1.4426950408889634f;
constexpr float THR = 8.0f;
constexpr unsigned WS_FLAGS = 8u << 20, WS_EPOCH = WS_FLAGS + 65536u, WS_FLAGS2 = 9u << 20, WS_PART = 16u << 20;
constexpr u64 EPOCH_INC = 0x9E3779B97F4A7C15ull;
constexpr unsigned SPIN_CAP = 1u << 22;
constexpr int SC1 = 16, NT = 2;

#define STAMP(i) do {} while (0)
#define LS() do {} while (0)

DI unsigned rfl(unsigned x) { return __builtin_amdgcn_readfirstlane(x); }
DI unsigned cvtpk(float lo, float hi) { f32x2 v = {lo, hi}; bf16x2 b = __builtin_convertvector(v, bf16x2); return __builtin_bit_cast(unsigned, b); }
DI bf16x8 pack8(const f32x16& x, int s) {
  u32x4 w = {cvtpk(x[8 * s + 0], x[8 * s + 1]), cvtpk(x[8 * s + 2], x[8 * s + 3]), cvtpk(x[8 * s + 4], x[8 * s + 5]), cvtpk(x[8 * s + 6], x[8 * s + 7])};
  return __builtin_bit_cast(bf16x8, w);
}
DI void dma16(__amdgpu_buffer_rsrc_t rs, unsigned voff, unsigned soff, unsigned ldsaddr) {
  asm volatile("s_mov_b32 m0, %0\n\ts_nop 0\n\tbuffer_load_dwordx4 %1, %2, %3 offen lds" ::"s"(ldsaddr), "v"(voff), "s"(rs), "s"(soff) : "memory");
}
#define DMA_PC(V, NTS) "buffer_load_dwordx4 " V ", %4, %0 offen " NTS "lds\n\t"
#define DMA_2(VA, VB, NTS) DMA_PC(VA, NTS) "s_add_u32 m0, m0, 0x400\n\ts_add_u32 %0, %0, %5\n\t" DMA_PC(VB, NTS) "s_add_u32 m0, m0, 0x400\n\ts_add_u32 %0, %0, %6\n\t"
template <int SEL> DI void dma_tile(__amdgpu_buffer_rsrc_t rs, unsigned voe, unsigned voo, unsigned soff, unsigned p1, unsigned p2, unsigned ldsaddr) {
  const unsigned d2 = p2 - p1;
  if constexpr (SEL == 0)
    asm volatile("s_mov_b32 m0, %1\n\ts_nop 0\n\t" DMA_2("%2", "%3", "") DMA_2("%2", "%3", "") DMA_2("%2", "%3", "") DMA_2("%2", "%3", "") DMA_2("%2", "%3", "") DMA_2("%2", "%3", "") DMA_2("%2", "%3", "") DMA_2("%2", "%3", "")
                 : "+&s"(soff) : "s"(ldsaddr), "v"(voe), "v"(voo), "s"(rs), "s"(p1), "s"(d2) : "memory", "scc");
  else
    asm volatile("s_mov_b32 m0, %1\n\ts_nop 0\n\t" DMA_2("%2", "%2", "nt ") DMA_2("%3", "%3", "nt ") DMA_2("%2", "%2", "nt ") DMA_2("%3", "%3", "nt ") DMA_2("%2", "%2", "nt ") DMA_2("%3", "%3", "nt ") DMA_2("%2", "%2", "nt ") DMA_2("%3", "%3", "nt ")
                 : "+&s"(soff) : "s"(ldsaddr), "v"(voe), "v"(voo), "s"(rs), "s"(p1), "s"(d2) : "memory", "scc");
}
DI bf16x8 rd128(const LAS char* p) { return *(const LAS bf16x8*)p; }
DI bf16x8 rdtr(const LAS char* lo, const LAS char* up) {
  s16x4 a = __builtin_amdgcn_ds_read_tr16_b64_v4i16((LAS s16x4*)lo);
  s16x4 b = __builtin_amdgcn_ds_read_tr16_b64_v4i16((LAS s16x4*)up);
  s16x8 c = __builtin_shufflevector(a, b, 0, 1, 2, 3, 4, 5, 6, 7);
  return __builtin_bit_cast(bf16x8, c);
}
#define BAR() asm volatile("s_barrier" ::: "memory")
#define BAR_L() asm volatile("s_waitcnt lgkmcnt(0)\n\ts_barrier" ::: "memory")
#define WAITV(N) asm volatile("s_waitcnt vmcnt(" #N ")" ::: "memory")
#define WAITV_BAR(N) asm volatile("s_waitcnt vmcnt(" #N ")\n\ts_barrier" ::: "memory")
#define MX3(a, b, c) __builtin_fmaxf(__builtin_fmaxf((a), (b)), (c))
DI float both_max(float x) { auto r = __builtin_amdgcn_permlane32_swap(__float_as_uint(x), __float_as_uint(x), false, false); return __builtin_fmaxf(__uint_as_float(r[0]), __uint_as_float(r[1])); }
DI float both_sum(float x) { auto r = __builtin_amdgcn_permlane32_swap(__float_as_uint(x), __float_as_uint(x), false, false); return __uint_as_float(r[0]) + __uint_as_float(r[1]); }
DI void mfma_acc(f32x16& d, const bf16x8& a, const bf16x8& b) { asm volatile("v_mfma_f32_32x32x16_bf16 %0, %1, %2, %0" : "+v"(d) : "v"(a), "v"(b) : "memory"); }
DI void mfma_new(f32x16& d, const bf16x8& a, const bf16x8& b, const f32x16& c) { asm volatile("v_mfma_f32_32x32x16_bf16 %0, %1, %2, %3" : "=&v"(d) : "v"(a), "v"(b), "v"(c) : "memory"); }
DI float aexp(float x) { asm volatile("v_exp_f32 %0, %0" : "+v"(x) :: "memory"); return x; }
DI unsigned acvt(float a, float b) { unsigned r; asm volatile("v_cvt_pk_bf16_f32 %0, %1, %2" : "=v"(r) : "v"(a), "v"(b) : "memory"); return r; }
DI void aadd1(float& l, float a) { asm volatile("v_add_f32 %0, %0, %1" : "+v"(l) : "v"(a) : "memory"); }
DI void aadd2(float& l, float a, float b) { asm volatile("v_add_f32 %0, %0, %1\n\tv_add_f32 %0, %0, %2" : "+&v"(l) : "v"(a), "v"(b) : "memory"); }
DI void aadd3(float& l, float a, float b, float c) { asm volatile("v_add_f32 %0, %0, %1\n\tv_add_f32 %0, %0, %2\n\tv_add_f32 %0, %0, %3" : "+&v"(l) : "v"(a), "v"(b), "v"(c) : "memory"); }
DI void aadd4(float& l, float a, float b, float c, float d) { asm volatile("v_add_f32 %0, %0, %1\n\tv_add_f32 %0, %0, %2\n\tv_add_f32 %0, %0, %3\n\tv_add_f32 %0, %0, %4" : "+&v"(l) : "v"(a), "v"(b), "v"(c), "v"(d) : "memory"); }
DI float amax7(float a, float b, float c, float d, float e, float f, float g) { float r; asm volatile("v_max3_f32 %0, %1, %2, %3\n\tv_max3_f32 %0, %0, %4, %5\n\tv_max3_f32 %0, %0, %6, %7" : "=&v"(r) : "v"(a), "v"(b), "v"(c), "v"(d), "v"(e), "v"(f), "v"(g) : "memory"); return r; }
DI void amax6(float& r, float a, float b, float c, float d, float e, float f) { asm volatile("v_max3_f32 %0, %0, %1, %2\n\tv_max3_f32 %0, %0, %3, %4\n\tv_max3_f32 %0, %0, %5, %6" : "+&v"(r) : "v"(a), "v"(b), "v"(c), "v"(d), "v"(e), "v"(f) : "memory"); }
DI void amax8(float& r, float a, float b, float c, float d, float e, float f, float g, float h) { asm volatile("v_max3_f32 %0, %0, %1, %2\n\tv_max3_f32 %0, %0, %3, %4\n\tv_max3_f32 %0, %0, %5, %6\n\tv_max3_f32 %0, %0, %7, %8" : "+&v"(r) : "v"(a), "v"(b), "v"(c), "v"(d), "v"(e), "v"(f), "v"(g), "v"(h) : "memory"); }
DI void amax3z(float& r, float a, float b, float c) { asm volatile("v_max3_f32 %0, %0, %1, %2\n\tv_max3_f32 %0, %0, %3, 0" : "+&v"(r) : "v"(a), "v"(b), "v"(c) : "memory"); }
DI int nxt5(int s) { return s == 4 * SLOT ? 0 : s + SLOT; }
struct Tile { u32x4 v[16]; };
DI void tile_init(Tile& t) {
#pragma unroll
  for (int p = 0; p < 16; ++p) asm volatile("" : "=v"(t.v[p]));
}
template <int AUX, int IMM> DI void ld16(u32x4& d, __amdgpu_buffer_rsrc_t rs, unsigned voff, unsigned soff) {
  if constexpr (AUX == NT) asm volatile("buffer_load_dwordx4 %0, %1, %2, %3 offen offset:%4 nt" : "+v"(d) : "v"(voff), "s"(rs), "s"(soff), "i"(IMM) : "memory");
  else                     asm volatile("buffer_load_dwordx4 %0, %1, %2, %3 offen offset:%4" : "+v"(d) : "v"(voff), "s"(rs), "s"(soff), "i"(IMM) : "memory");
}
template <int P> DI void wr16(const u32x4& d, unsigned addr) { asm volatile("ds_write_b128 %0, %1 offset:%2" ::"v"(addr), "v"(d), "i"(P * 1024) : "memory"); }
DI void tile_wr(const Tile& t, unsigned addr) {
  wr16<0>(t.v[0], addr); wr16<1>(t.v[1], addr); wr16<2>(t.v[2], addr); wr16<3>(t.v[3], addr); wr16<4>(t.v[4], addr); wr16<5>(t.v[5], addr); wr16<6>(t.v[6], addr); wr16<7>(t.v[7], addr);
  wr16<8>(t.v[8], addr); wr16<9>(t.v[9], addr); wr16<10>(t.v[10], addr); wr16<11>(t.v[11], addr); wr16<12>(t.v[12], addr); wr16<13>(t.v[13], addr); wr16<14>(t.v[14], addr); wr16<15>(t.v[15], addr);
}

__global__ __launch_bounds__(512, 2) void mha_fused(const char* x, const char* kc, const char* vc, const char* wq, const char* wo, char* out, char* ws) {
  __shared__ __attribute__((aligned(16))) char lds[LDS_BYTES];
  u64 pc0; asm volatile("s_getpc_b64 %0" : "=s"(pc0));
  u64 tk0 = __builtin_amdgcn_s_memrealtime(); u64 tkx = 0;
  const int tid = threadIdx.x, lane = tid & 63;
  const int wid = rfl(tid >> 6);
  const int wg = blockIdx.x;
  const int xcd = wg & 7, j = wg >> 3;
  const int b = j & 7, h = xcd * 4 + (j >> 3);
  const bool late = (j ^ (j >> 3)) & 1;
  const int kq = xcd >> 1, ob = (xcd & 1) * 4 + (j >> 3), nb = j & 7;
  auto hmap = [&](int p) { return 8 * kq + 2 * (p & 3) + ((ob & 1) ^ (p >> 2)); };
  const unsigned lds0 = (unsigned)(size_t)(LAS char*)lds;

  const __amdgpu_buffer_rsrc_t rcode = SRD(pc0 & ~255ull, CODE_KB * 1024);
  if ((wid >> 1) != 2) dma16(rcode, lane * 16u, ((j & 3) * 6 + (wid < 4 ? wid : wid - 2)) * 1024u, lds0 + 4 * SLOT + wid * 1024);
  asm volatile("" : "+s"(x), "+s"(kc), "+s"(vc), "+s"(wq), "+s"(wo), "+s"(out), "+s"(ws));
  GAS u64* flags = (GAS u64*)(ws + WS_FLAGS);

  if (__builtin_expect(wid >= 4, 1)) {
    const unsigned r8 = (lane >> 2) & 7, sub = lane >> 5, c4 = lane & 3;
    const unsigned hb = wid & 1;
    const unsigned xe = hb ? c4 : (c4 ^ (r8 >> 2)), xo = hb ? c4 : (c4 ^ (2 + (r8 >> 2)));
    const unsigned ldh = lds0 + hb * 16384u;
    if (__builtin_expect(wid >= 6, 0)) {
      const __amdgpu_buffer_rsrc_t rs = SRD((hb ? vc : kc) + (size_t)(b * NH + h) * SK * DH * 2, SK * DH * 2);
      const unsigned voe = r8 * 256u + sub * 64u + 16u * xe, voo = r8 * 256u + sub * 64u + 16u * xo;
      const int krot = (j + 5 * xcd) & 15;
      auto tile_dma = [&](int t, int so) { dma_tile<1>(rs, voe, voo, ((t + krot) & 15) * 16384u, 128u, 2048u, ldh + so); };
      const __amdgpu_buffer_rsrc_t rwo = SRD(wo + nb * 256, NH * DH * DM * 2 - nb * 256);
      const unsigned vw0 = (hb * 64u + r8) * 2048u + 16u * c4 + 64u * ((kq + sub) & 3), vw1 = (hb * 64u + r8) * 2048u + 16u * c4 + 64u * ((kq + 2 + sub) & 3);
      auto w_dma = [&](int p, int so) { dma_tile<0>(rwo, vw0, vw1, (unsigned)hmap(p) * (128u * 2048u), 0u, 16384u, ldh + so); };
      WAITV(0);
      int sl = 3 * SLOT;
      int n0 = 5;
      if (late) {
        const unsigned wdst = ldh + lane * 16u;
        Tile R0, R1, R2;
        tile_init(R0); tile_init(R1); tile_init(R2);
        auto tile_ld = [&](Tile& R, int t) {
#pragma unroll
          for (int q = 0; q < 4; ++q) {
            const unsigned so = ((t + krot) & 15) * 16384u + q * 4096u;
            ld16<NT, 0>(R.v[4 * q], rs, voe, so); ld16<NT, 128>(R.v[4 * q + 1], rs, voe, so);
            ld16<NT, 2048>(R.v[4 * q + 2], rs, voo, so); ld16<NT, 2048 + 128>(R.v[4 * q + 3], rs, voo, so);
          }
        };
#pragma unroll 1
        for (int c = -1; c <= KV_B; ++c) BAR();
        tile_dma(0, 3 * SLOT); tile_dma(1, 4 * SLOT); tile_ld(R0, 2);
        WAITV(32); tile_ld(R1, 3);
        WAITV(32); tile_ld(R2, 4);
#pragma unroll 1
        for (int c = KV_B + 1; c <= 14; ++c) BAR();
        WAITV_BAR(0);
        tile_wr(R0, wdst);
        BAR_L(); tile_wr(R1, wdst + SLOT);
        if (hb) BAR_L();
        tile_wr(R2, wdst + 2 * SLOT);
        asm volatile("s_waitcnt lgkmcnt(0)" ::: "memory");
      } else {
#pragma unroll 1
        for (int c = -1; c <= 12 + (int)hb; ++c) BAR();
        __builtin_amdgcn_s_sleep(6);
        tile_dma(0, sl); sl = nxt5(sl);
        n0 = 1;
      }
#pragma unroll 1
      for (int n = n0; n <= 15; ++n) { WAITV_BAR(32); tile_dma(n, sl); sl = nxt5(sl); }
      if (!hb) {
        WAITV_BAR(32); WAITV_BAR(16); w_dma(0, sl); sl = nxt5(sl);
        WAITV_BAR(16); w_dma(1, sl); sl = nxt5(sl);
        BAR(); w_dma(2, sl); sl = nxt5(sl);
      } else {
#pragma unroll 1
        for (int s = 0; s <= 2; ++s) { WAITV_BAR(32); w_dma(s, sl); sl = nxt5(sl); }
      }
      WAITV_BAR(32);
      WAITV(31);
      __builtin_amdgcn_s_sleep(8);
      w_dma(3, sl); sl = nxt5(sl); w_dma(4, sl);
#pragma unroll 1
      for (int p = 5; p < 8; ++p) { BAR(); WAITV_BAR(47); sl = nxt5(sl); w_dma(p, sl); }
      BAR(); WAITV_BAR(47); BAR(); WAITV_BAR(32); BAR(); WAITV_BAR(16); BAR(); WAITV_BAR(0); BAR(); BAR();
      return;
    }
    u64 tag = 0;
    {
      const int qrot = (2 * (j & 7) + 4 * (j >> 3)) & 15;
      u64 qbase = hb ? (u64)wq : (u64)x; unsigned qoff = hb ? h * DH * 2 : b * SQ * DM * 2, qn = hb ? DM * NH * DH * 2 : (b + 1) * SQ * DM * 2;
      unsigned rsh = hb ? 13u : 11u, cstr = hb ? 64u * 8192u : 128u, p1 = hb ? 128u : 16384u, p2 = hb ? 65536u : 32768u;
      asm volatile("" : "+s"(qbase), "+s"(qoff), "+s"(qn), "+s"(rsh), "+s"(cstr), "+s"(p1), "+s"(p2));
      const __amdgpu_buffer_rsrc_t rs = SRD(qbase, qn);
      const unsigned voe = (r8 << rsh) + sub * 64u + 16u * xe, voo = (r8 << rsh) + sub * 64u + 16u * xo;
      int sl = 2 * SLOT;
      tag = __hip_atomic_load((GAS u64*)(ws + WS_EPOCH), RLX_AGENT);
#pragma unroll 1
      for (int c = 0; c < 16; ++c) {
        dma_tile<0>(rs, voe, voo, qoff + ((c + qrot) & 15) * cstr, p1, p2, ldh + sl); sl = sl == 2 * SLOT ? 0 : sl + SLOT;
        if (c == 2) { WAITV_BAR(32); LS(); }
        if (c >= 2) { WAITV_BAR(16); LS(); }
      }
      WAITV_BAR(0); LS(); BAR(); LS();
    }
    tag += EPOCH_INC;
    {
      u64 seen = 0;
      const GAS u64* myflag = flags + (((ob * 4 + kq) * 8 + nb) * 32 + (lane & 31));
      auto gate = [&](u64 need) {
        if (hb) return;
#pragma unroll 1
        for (unsigned spins = 0; (seen & need) != need && spins < SPIN_CAP; ++spins) {
          seen = __ballot(__hip_atomic_load(myflag, RLX_AGENT) == tag);
          if ((seen & need) != need) __builtin_amdgcn_s_sleep(1);
        }
      };
      auto hbit = [&](int p) { return 0x01010101ull << (hmap(p) & 7); };
#pragma unroll 1
      for (int t = 0; t <= 10; ++t) { BAR(); LS(); }
      u32x4 d0; asm volatile("" : "=v"(d0));
      ld16<0, 0>(d0, rcode, lane * 16u, ((j >> 1) * 2 + hb) * 1024u);
#pragma unroll 1
      for (int t = 11; t <= 14; ++t) { BAR(); LS(); }
      WAITV_BAR(0); LS();
      asm volatile("" ::"v"(d0));
      gate(hbit(0) | hbit(1)); BAR(); LS();
#pragma unroll 1
      for (int m = 0; m < 8; ++m) { if (m < 6) gate(hbit(m + 2)); BAR(); BAR(); LS(); }
    }
    if (wg == 0 && wid == 4) {
#pragma unroll 1
      for (unsigned spins = 0; spins < SPIN_CAP; ++spins) {
        bool ok = true;
#pragma unroll
        for (int i = 0; i < 16; ++i) ok &= __hip_atomic_load(flags + ((2 * i + (lane >> 5)) * 256 + (lane & 31)), RLX_AGENT) == tag;
        if (__all(ok)) break;
        __builtin_amdgcn_s_sleep(2);
      }
      if (lane == 0) __hip_atomic_store((GAS u64*)(ws + WS_EPOCH), tag, RLX_AGENT);
    }
    return;
  }
  STAMP(0);
  const u64 tag = __hip_atomic_load((GAS u64*)(ws + WS_EPOCH), RLX_AGENT) + EPOCH_INC;
  const int r = lane & 31, hh = lane >> 5, qb = wid;
  const int tq = (lane >> 2) & 3, tp = lane & 3, blk = (lane >> 4) & 1;
  const LAS char* L = (const LAS char*)lds;
  const int rowe = 64 * (r & 7) + 16 * (hh ^ ((r >> 2) & 3));
  const int kbase_e = 2048 * (r >> 3) + rowe, kbase_o = kbase_e ^ 32;
  const int tbase = hh * 256 + tq * 64 + blk * 32 + tp * 8;
  bf16x8 qf[8];
  bf16x8 kf[16], vf[16];
  f32x16 acc[4] = {};
#define KRD(i, SP) kf[i] = rd128((SP) + ((((i) >> 1) & 1) ? kbase_o : kbase_e) + 512 * ((i) >> 2) + 8192 * ((i) & 1))
  {
    const int xbase_e = 1024 * (4 * qb + (r >> 3)) + rowe, xbase_o = xbase_e ^ 32;
    const int pp = (tp == 1) ? 2 : (tp == 2) ? 1 : tp;
    const int wbase = 16384 + hh * 2048 + tq * 64 + blk * 32 + pp * 8;
    bf16x8 xf[4], wf[16];
    int sl = 2 * SLOT;
#define WRD(i, SP) wf[i] = rdtr((SP) + wbase + 4096 * ((i) >> 2) + 512 * ((i) & 3), (SP) + wbase + 4096 * ((i) >> 2) + 512 * ((i) & 3) + 256)
#define XRD(ks, SP) xf[ks] = rd128((SP) + (((ks) & 1) ? xbase_o : xbase_e) + 512 * ((ks) >> 1))
#define GQ(g, BODY) do { acc[(g) & 3] = MFMA(wf[g], xf[(g) >> 2], acc[(g) & 3]); BODY; SBAR(); } while (0)
    __builtin_amdgcn_s_waitcnt(0xC07F);
    WAITV_BAR(0);
    STAMP(1);
    { const LAS char* S = L + sl; XRD(0, S); WRD(0, S); SBAR(); WRD(1, S); WRD(2, S); SBAR(); WRD(3, S); XRD(1, S); SBAR(); WRD(4, S); SBAR(); WRD(5, S); SBAR(); }
#define QCHUNK(LAST) do { const LAS char* S = L + sl; sl = sl == 2 * SLOT ? 0 : sl + SLOT; const LAS char* N = (LAST) ? L + 3 * SLOT : L + sl; SBAR(); \
      GQ(0, { WRD(6, S); XRD(2, S); }); GQ(1, { WRD(7, S); XRD(3, S); }); GQ(2, { WRD(8, S); }); GQ(3, { WRD(9, S); WRD(10, S); }); GQ(4, WRD(11, S)); GQ(5, { WRD(12, S); WRD(13, S); }); \
      GQ(6, { WRD(14, S); }); GQ(7, WRD(15, S)); GQ(8, {}); GQ(9, {}); GQ(10, {}); \
      BAR_L(); SBAR(); GQ(11, {}); \
      if (!(LAST)) { XRD(0, N); WRD(0, N); SBAR(); GQ(12, { WRD(1, N); WRD(2, N); }); GQ(13, { WRD(3, N); XRD(1, N); }); GQ(14, { WRD(4, N); }); GQ(15, WRD(5, N)); } \
      else { GQ(12, { KRD(0, N); KRD(1, N); }); GQ(13, { KRD(2, N); KRD(3, N); }); GQ(14, { KRD(4, N); KRD(5, N); }); GQ(15, { KRD(6, N); KRD(7, N); }); } } while (0)
#pragma unroll 1
    for (int c = 0; c < 15; ++c) QCHUNK(false);
    QCHUNK(true);
#pragma unroll
    for (int d = 0; d < 4; ++d)
#pragma unroll
      for (int i = 0; i < 16; ++i) acc[d][i] *= C2;
#pragma unroll
    for (int s = 0; s < 8; ++s) qf[s] = pack8(acc[s >> 1], s & 1);
  }
  STAMP(2);
  {
    f32x16 o[4] = {};
    float l = 0.f;
    f32x16 sA0, sA1, sB0, sB1, negm;
    u32x4 pw[4];
    const f32x16 zero16 = {};
    int sk = 3 * SLOT;
    int sv = 3 * SLOT + 16384;
#define VRD(i, SP) vf[i] = rdtr((SP) + tbase + 4096 * ((i) >> 2) + 512 * ((i) & 3), (SP) + tbase + 4096 * ((i) >> 2) + 512 * ((i) & 3) + 2048)
#define EX(v) __builtin_amdgcn_exp2f(v)
#define PF(k) __builtin_bit_cast(bf16x8, pw[k])
#define LW(n) __builtin_amdgcn_s_waitcnt(0xC07F | ((n) << 8))
    {
      const LAS char* KP = L + sk;
#pragma unroll
      for (int i = 8; i < 16; ++i) KRD(i, KP);
      sA0 = MFMA(kf[0], qf[0], zero16); sA1 = MFMA(kf[1], qf[0], zero16);
#pragma unroll
      for (int s = 1; s < 8; ++s) { sA0 = MFMA(kf[2 * s], qf[s], sA0); sA1 = MFMA(kf[2 * s + 1], qf[s], sA1); }
      BAR_L();
      STAMP(3);
      sk = nxt5(sk);
      const LAS char* KN = L + sk;
      KRD(0, KN); SBAR(); KRD(1, KN); SBAR(); KRD(2, KN); SBAR(); KRD(3, KN); SBAR();
      float a = MX3(sA0[0], sA0[1], sA1[0]); a = MX3(a, sA1[1], sA0[2]);
#pragma unroll
      for (int i = 3; i < 16; i += 2) a = MX3(a, sA0[i], (i + 1 < 16) ? sA0[i + 1] : sA0[i]);
#pragma unroll
      for (int i = 2; i < 16; i += 2) a = MX3(a, sA1[i], sA1[i + 1]);
      const float m = both_max(a);
#pragma unroll
      for (int i = 0; i < 16; ++i) { sA0[i] -= m; sA1[i] -= m; negm[i] = -m; }
#pragma unroll
      for (int i = 0; i < 13; ++i) sA0[i] = EX(sA0[i]);
    }
#define GA(i, X, BODY) do { if ((i) < 2) mfma_new(X, kf[i], qf[0], negm); else mfma_acc(X, kf[i], qf[(i) >> 1]); if ((i) + 4 < 16) { KRD(((i) + 4) & 15, KP_); } else { VRD((i) & 3, VP_); } BODY; } while (0)
#define E1(Y, e) do { Y[e] = aexp(Y[e]); } while (0)
#define E2(Y, e) do { Y[e] = aexp(Y[e]); Y[e + 1] = aexp(Y[e + 1]); } while (0)
#define S1(Y, e) aadd1(l, Y[e])
#define S2(Y, e) aadd2(l, Y[e], Y[e + 1])
#define S3(Y, e) aadd3(l, Y[e], Y[e + 1], Y[e + 2])
#define S4(Y, e) aadd4(l, Y[e], Y[e + 1], Y[e + 2], Y[e + 3])
#define PK(k, d, Y, e) do { pw[k][d] = acvt(Y[e], Y[e + 1]); } while (0)
#define PHASE_A(X0, X1, Y0, Y1) do { const LAS char* KP_ = L + sk; const LAS char* VP_ = L + sv; \
      LW(2); GA(0,  X0, { E2(Y0, 13); }); \
             GA(1,  X1, { PK(0, 0, Y0, 0); E1(Y0, 15); E1(Y1, 0); }); \
      LW(2); GA(2,  X0, { E2(Y1, 1); }); \
             GA(3,  X1, { PK(0, 1, Y0, 2); E2(Y1, 3); }); \
      LW(2); GA(4,  X0, { E2(Y1, 5); asm volatile("" ::"v"(negm));   }); \
             GA(5,  X1, { PK(0, 2, Y0, 4); E2(Y1, 7); }); \
      LW(2); GA(6,  X0, { E2(Y1, 9); }); \
             GA(7,  X1, { PK(0, 3, Y0, 6); E2(Y1, 11); }); \
      LW(2); GA(8,  X0, { E2(Y1, 13); }); \
             GA(9,  X1, { PK(1, 0, Y0, 8); S2(Y0, 0); E1(Y1, 15); }); \
      LW(2); GA(10, X0, { PK(1, 1, Y0, 10); S3(Y0, 2); }); \
             GA(11, X1, { PK(1, 2, Y0, 12); S4(Y0, 5); }); \
      LW(2); GA(12, X0, { PK(1, 3, Y0, 14); S2(Y0, 9); }); \
             GA(13, X1, { S4(Y0, 11); }); \
      LW(4); GA(14, X0, { aadd3(l, Y0[15], Y1[0], Y1[1]); }); \
             GA(15, X1, { S2(Y1, 2); }); \
      } while (0)
#define GB(i, NEXTK, BODY) do { mfma_acc(o[(i) & 3], vf[i], PF((i) >> 2)); if ((i) + 4 < 16) { VRD(((i) + 4) & 15, VP_); } else if (NEXTK) { KRD((i) & 3, KN_); } BODY; } while (0)
#define PHASE_B(X0, X1, Y1, NEXTK) do { const LAS char* VP_ = L + sv; const LAS char* KN_ = L + sk; float a_; float alpha_ = 1.f; bool resc_ = false; \
      LW(4); GB(0, NEXTK, { PK(2, 0, Y1, 0); S2(Y1, 4); }); \
             GB(1, NEXTK, { PK(2, 1, Y1, 2); PK(2, 2, Y1, 4); PK(2, 3, Y1, 6); S1(Y1, 6); }); \
      LW(4); GB(2, NEXTK, { a_ = amax7(X0[0], X0[1], X0[2], X0[3], X0[4], X0[5], X0[6]); }); \
             GB(3, NEXTK, { amax8(a_, X0[7], X0[8], X0[9], X0[10], X0[11], X0[12], X0[13], X0[14]); }); \
      LW(4); GB(4, NEXTK, { amax6(a_, X0[15], X1[0], X1[1], X1[2], X1[3], X1[4]); }); \
             GB(5, NEXTK, { amax8(a_, X1[5], X1[6], X1[7], X1[8], X1[9], X1[10], X1[11], X1[12]); }); \
      LW(4); GB(6, NEXTK, { amax3z(a_, X1[13], X1[14], X1[15]); S1(Y1, 7); }); \
             GB(7, NEXTK, { S2(Y1, 8); }); \
      if (__builtin_expect(__any(a_ > THR), 0)) { const float dl_ = both_max(a_); alpha_ = __builtin_amdgcn_exp2f(-dl_); resc_ = true; \
        _Pragma("unroll") for (int i_ = 0; i_ < 16; ++i_) { X0[i_] -= dl_; X1[i_] -= dl_; negm[i_] -= dl_; } } \
      LW(4); GB(8,  NEXTK, { PK(3, 0, Y1, 8); PK(3, 1, Y1, 10); S1(Y1, 10); }); \
             GB(9,  NEXTK, { PK(3, 2, Y1, 12); PK(3, 3, Y1, 14); E1(X0, 0); }); \
      LW(4); GB(10, NEXTK, { S1(Y1, 11); E1(X0, 1); }); \
             GB(11, NEXTK, { E2(X0, 2); }); \
      LW(4); GB(12, NEXTK, { S1(Y1, 12); E2(X0, 4); }); \
             GB(13, NEXTK, { S1(Y1, 13); E2(X0, 6); }); \
      if (NEXTK) LW(2); else LW(0); \
             GB(14, NEXTK, { S1(Y1, 14); E2(X0, 8); }); \
             GB(15, NEXTK, { S1(Y1, 15); E2(X0, 10); E1(X0, 12); }); \
      if (__builtin_expect(resc_, 0)) { asm volatile("s_nop 7\n\ts_nop 7" : "+v"(o[0]), "+v"(o[1]), "+v"(o[2]), "+v"(o[3]) :: "memory"); \
        l *= alpha_; _Pragma("unroll") for (int d_ = 0; d_ < 4; ++d_) _Pragma("unroll") for (int i_ = 0; i_ < 16; ++i_) o[d_][i_] *= alpha_; } \
      sv = sv == 4 * SLOT + 16384 ? 16384 : sv + SLOT; } while (0)
    asm volatile("s_nop 1" : "+v"(negm), "+v"(sA0), "+v"(sA1) :: "memory");
#pragma unroll 1
    for (int t = 1; t < 15; t += 2) {
      PHASE_A(sB0, sB1, sA0, sA1); BAR(); sk = nxt5(sk); PHASE_B(sB0, sB1, sA1, true);
      PHASE_A(sA0, sA1, sB0, sB1); BAR(); sk = nxt5(sk); PHASE_B(sA0, sA1, sB1, true);
    }
    PHASE_A(sB0, sB1, sA0, sA1); BAR(); PHASE_B(sB0, sB1, sA1, false);
    {
      const LAS char* VP_ = L + sv;
#pragma unroll
      for (int i = 0; i < 16; ++i) VRD(i, VP_);
#pragma unroll
      for (int i = 13; i < 16; ++i) sB0[i] = EX(sB0[i]);
      const bf16x8 p0 = pack8(sB0, 0), p1 = pack8(sB0, 1);
#pragma unroll
      for (int db = 0; db < 4; ++db) o[db] = MFMA(vf[db], p0, o[db]);
#pragma unroll
      for (int i = 0; i < 16; ++i) sB1[i] = EX(sB1[i]);
#pragma unroll
      for (int db = 0; db < 4; ++db) o[db] = MFMA(vf[4 + db], p1, o[db]);
      const bf16x8 p2 = pack8(sB1, 0), p3 = pack8(sB1, 1);
#pragma unroll
      for (int db = 0; db < 4; ++db) o[db] = MFMA(vf[8 + db], p2, o[db]);
#pragma unroll
      for (int i = 0; i < 16; ++i) l += sB0[i];
#pragma unroll
      for (int db = 0; db < 4; ++db) o[db] = MFMA(vf[12 + db], p3, o[db]);
#pragma unroll
      for (int i = 0; i < 16; ++i) l += sB1[i];
      l = both_sum(l);
    }
    { asm volatile("s_memrealtime %0\n\ts_waitcnt lgkmcnt(0)" : "=s"(tk0) :: "memory"); }
    const float inv = __builtin_amdgcn_rcpf(l);
    const __amdgpu_buffer_rsrc_t rs = SRD(ws + (unsigned)(((b * 4 + qb) * 256 + h * 8) * 1024), 8192);
#pragma unroll
    for (int db = 0; db < 4; ++db) {
#pragma unroll
      for (int i = 0; i < 16; ++i) o[db][i] *= inv;
#pragma unroll
      for (int s = 0; s < 2; ++s) __builtin_amdgcn_raw_buffer_store_b128(__builtin_bit_cast(u32x4, pack8(o[db], s)), rs, lane * 16 + (db * 2 + s) * 1024, 0, SC1);
    }
#pragma unroll
    for (int i = 0; i < 4; ++i) acc[i] = (f32x16){};
    asm volatile("s_waitcnt vmcnt(0)" : "+v"(acc[0]), "+v"(acc[1]), "+v"(acc[2]), "+v"(acc[3]) :: "memory");
    if (lane < 8) __hip_atomic_store(flags + (((b * 4 + (h >> 3)) * 8 + lane) * 32 + qb * 8 + (h & 7)), tag, RLX_AGENT);
  }
  { asm volatile("s_memrealtime %0\n\ts_waitcnt lgkmcnt(0)" : "=s"(tkx) :: "memory"); }
  {
    BAR_L();
    STAMP(9);
    const __amdgpu_buffer_rsrc_t rso = SRD(ws + (size_t)(ob * 4 + qb) * 262144, 262144);
    u32x4 bq[2][8];
    bf16x8 wf[16];
    int sw = 4 * SLOT;
#define OL1(lb, lh, so_, i_) bq[lb][4 * (lh) + (i_)] = __builtin_amdgcn_raw_buffer_load_b128(rso, lane * 16 + (i_) * 1024, so_, SC1)
#define OLD4(buf, p, hf) do { const unsigned so_ = (unsigned)hmap(p) * 8192u + (hf) * 4096u; _Pragma("unroll") for (int i_ = 0; i_ < 4; ++i_) { OL1(buf, hf, so_, i_); SBAR(); } } while (0)
#define ORD(i, SP) wf[i] = rdtr((SP) + tbase + 4096 * ((i) >> 2) + 512 * ((i) & 3), (SP) + tbase + 4096 * ((i) >> 2) + 512 * ((i) & 3) + 2048)
#define GO(buf, hf, g, BODY) do { acc[(g) & 3] = MFMA(wf[g], __builtin_bit_cast(bf16x8, bq[buf][4 * (hf) + ((g) >> 2)]), acc[(g) & 3]); BODY; SBAR(); } while (0)
#define OHALF(buf, hf, S, N, SYNC, MORE, LD, lb, lh, lp) do { unsigned so_ = 0; SBAR(); \
      GO(buf, hf, 0, { ORD(6, S); so_ = (unsigned)hmap(lp) * 8192u + (lh) * 4096u + (unsigned)((lp) >> 3) * 0x40000000u; asm volatile("" : "+s"(so_)); }); GO(buf, hf, 1, { ORD(7, S); if (LD) OL1(lb, lh, so_, 0); }); \
      GO(buf, hf, 2, { ORD(8, S); if (LD) OL1(lb, lh, so_, 1); }); GO(buf, hf, 3, { ORD(9, S); if (LD) OL1(lb, lh, so_, 2); }); GO(buf, hf, 4, { ORD(10, S); if (LD) OL1(lb, lh, so_, 3); }); \
      GO(buf, hf, 5, { ORD(11, S); ORD(12, S); }); GO(buf, hf, 6, { ORD(13, S); ORD(14, S); }); GO(buf, hf, 7, ORD(15, S)); GO(buf, hf, 8, {}); GO(buf, hf, 9, {}); GO(buf, hf, 10, {}); \
      if ((SYNC) == 1) { BAR_L(); SBAR(); } else if ((SYNC) == 2) { BAR(); SBAR(); } \
      GO(buf, hf, 11, if (MORE) ORD(0, N)); GO(buf, hf, 12, if (MORE) { ORD(1, N); ORD(2, N); }); GO(buf, hf, 13, if (MORE) ORD(3, N)); GO(buf, hf, 14, if (MORE) ORD(4, N)); GO(buf, hf, 15, if (MORE) ORD(5, N)); } while (0)
#define OSTAGE(buf, p, LAST, LD2) do { const LAS char* S_ = L + sw; sw = nxt5(sw); const LAS char* N_ = L + sw; \
      OHALF(buf, 0, S_, S_ + 16384, (LAST) ? 0 : 2, true, !(LAST), (buf) ^ 1, 1, (p) + 1); \
      OHALF(buf, 1, S_ + 16384, N_, (LAST) ? 0 : 1, !(LAST), LD2, buf, 0, (p) + 2); } while (0)
    OLD4(0, 0, 0); OLD4(0, 0, 1); OLD4(1, 1, 0);
    { const LAS char* S_ = L + sw; ORD(0, S_); SBAR(); ORD(1, S_); ORD(2, S_); SBAR(); ORD(3, S_); SBAR(); ORD(4, S_); SBAR(); ORD(5, S_); SBAR(); }
#pragma unroll 1
    for (int p = 0; p < 8; p += 2) { OSTAGE(0, p, false, true); OSTAGE(1, p + 1, false, true); }
    STAMP(10);
    const int tile = ob * 8 + nb;
    GAS u64* flags2 = (GAS u64*)(ws + WS_FLAGS2) + (tile * 16 + qb) * 16;
    {
      const __amdgpu_buffer_rsrc_t rp = SRD(ws + WS_PART + (size_t)tile * 131072 + (kq * 4 + qb) * 2048, 131072);
#pragma unroll
      for (int i = 1; i < 4; ++i)
#pragma unroll
        for (int g = 0; g < 2; ++g) __builtin_amdgcn_raw_buffer_store_b128(__builtin_bit_cast(u32x4, pack8(acc[i], g)), rp, lane * 16 + ((kq + i) & 3) * 32768u + g * 1024, 0, SC1);
      WAITV(0);
      if (lane < 4) __hip_atomic_store(flags2 + lane * 64 + kq, tag, RLX_AGENT);
    }
    STAMP(4);
#pragma unroll 1
    for (unsigned spins = 0; spins < SPIN_CAP; ++spins) {
      if (__all(__hip_atomic_load(flags2 + kq * 64 + (lane & 3), RLX_AGENT) == tag || (lane & 3) == kq)) break;
      __builtin_amdgcn_s_sleep(1);
    }
    __builtin_amdgcn_fence(__ATOMIC_ACQUIRE, "wavefront");
    STAMP(5);
    {
      const __amdgpu_buffer_rsrc_t rp = SRD(ws + WS_PART + (size_t)tile * 131072 + kq * 32768 + qb * 2048, 32768);
      u32x4 pv[3][2];
#pragma unroll
      for (int i = 1; i < 4; ++i)
#pragma unroll
        for (int g = 0; g < 2; ++g) pv[i - 1][g] = __builtin_amdgcn_raw_buffer_load_b128(rp, lane * 16 + g * 1024, ((kq + i) & 3) * 8192u, SC1);
      f32x16 sum = acc[0];
#pragma unroll
      for (int i = 0; i < 3; ++i)
#pragma unroll
        for (int e = 0; e < 8; ++e) { const unsigned w = pv[i][e >> 2][e & 3]; sum[2 * e] += __uint_as_float(w << 16); sum[2 * e + 1] += __uint_as_float(w & 0xffff0000u); }
      GAS char* orow = (GAS char*)out + (size_t)(ob * 128 + qb * 32 + r) * (DM * 2) + (nb * 128 + kq * 32) * 2 + hh * 16;
#pragma unroll
      for (int g = 0; g < 2; ++g) {
        unsigned a0 = cvtpk(sum[8 * g], sum[8 * g + 1]), a1 = cvtpk(sum[8 * g + 2], sum[8 * g + 3]);
        unsigned b0 = cvtpk(sum[8 * g + 4], sum[8 * g + 5]), b1 = cvtpk(sum[8 * g + 6], sum[8 * g + 7]);
        auto r0 = __builtin_amdgcn_permlane32_swap(a0, b0, false, false);
        auto r1 = __builtin_amdgcn_permlane32_swap(a1, b1, false, false);
        u32x4 w = {r0[0], r1[0], r0[1], r1[1]};
        *(GAS u32x4*)(orow + g * 32) = w;
      }
    }
    STAMP(11);
  }
  { const u64 te = __builtin_amdgcn_s_memrealtime(); while (__builtin_amdgcn_s_memrealtime() - te < 10 * (tkx - tk0)) __builtin_amdgcn_s_sleep(2); }
}

extern "C" void kernel_launch(void* const* d_in, const int* in_sizes, int n_in, void* d_out, int out_size, void* d_ws, size_t ws_size, hipStream_t stream) {
  mha_fused<<<256, 512, 0, stream>>>((const char*)d_in[0], (const char*)d_in[1], (const char*)d_in[2], (const char*)d_in[3], (const char*)d_in[4], (char*)d_out, (char*)d_ws);
}
```

```cpp
#include <hip/hip_runtime.h>
#include <hip/hip_bf16.h>

constexpr int KV_B = -1;
constexpr int THIN_TO = 8;
constexpr int UE = 7, UL = 5;
constexpr int CODE_BYTES = 24512;

typedef __bf16 bf16x8 __attribute__((ext_vector_type(8)));
typedef __bf16 bf16x2 __attribute__((ext_vector_type(2)));
typedef short s16x4 __attribute__((ext_vector_type(4)));
typedef short s16x8 __attribute__((ext_vector_type(8)));
typedef float f32x16 __attribute__((ext_vector_type(16)));
typedef float f32x4 __attribute__((ext_vector_type(4)));
typedef float f32x2 __attribute__((ext_vector_type(2)));
typedef unsigned u32x4 __attribute__((ext_vector_type(4)));
typedef unsigned long long u64;
#define LAS __attribute__((address_space(3)))
#define GAS __attribute__((address_space(1)))
#define DI __device__ __forceinline__
#define MFMA(a, b, c) __builtin_amdgcn_mfma_f32_32x32x16_bf16((a), (b), (c), 0, 0, 0)
#define SBAR() __builtin_amdgcn_sched_barrier(0)
#define RLX_AGENT __ATOMIC_RELAXED, __HIP_MEMORY_SCOPE_AGENT
#define SRD(p, n) __builtin_amdgcn_make_buffer_rsrc((void*)(p), 0, (n), 0x00020000)

constexpr int SQ = 128, DM = 1024, NH = 32, SK = 1024, DH = 128;
constexpr int SLOT = 32768, LDS_BYTES = 163840;
constexpr float C2 = 0.08838834764831845f * 1.4426950408889634f;
constexpr float THR = 8.0f;
constexpr unsigned WS_FLAGS = 8u << 20, WS_EPOCH = WS_FLAGS + 65536u, WS_FLAGS2 = 9u << 20, WS_PART = 16u << 20;
constexpr u64 EPOCH_INC = 0x9E3779B97F4A7C15ull;
constexpr unsigned SPIN_CAP = 1u << 17;
constexpr int SC1 = 16, NT = 2;


DI unsigned rfl(unsigned x) { return __builtin_amdgcn_readfirstlane(x); }
DI unsigned cvtpk(float lo, float hi) { f32x2 v = {lo, hi}; bf16x2 b = __builtin_convertvector(v, bf16x2); return __builtin_bit_cast(unsigned, b); }
DI bf16x8 pack8(const f32x16& x, int s) {
  u32x4 w = {cvtpk(x[8 * s + 0], x[8 * s + 1]), cvtpk(x[8 * s + 2], x[8 * s + 3]), cvtpk(x[8 * s + 4], x[8 * s + 5]), cvtpk(x[8 * s + 6], x[8 * s + 7])};
  return __builtin_bit_cast(bf16x8, w);
}
#define DMA_PC(V, NTS) "buffer_load_dwordx4 " V ", %4, %0 offen " NTS "lds\n\t"
#define DMA_2(VA, VB, NTS) DMA_PC(VA, NTS) "s_add_u32 m0, m0, 0x400\n\ts_add_u32 %0, %0, %5\n\t" DMA_PC(VB, NTS) "s_add_u32 m0, m0, 0x400\n\ts_add_u32 %0, %0, %6\n\t"
template <int SEL> DI void dma_tile(__amdgpu_buffer_rsrc_t rs, unsigned voe, unsigned voo, unsigned soff, unsigned p1, unsigned p2, unsigned ldsaddr) {
  const unsigned d2 = p2 - p1;
  if constexpr (SEL == 0)
    asm volatile("s_mov_b32 m0, %1\n\ts_nop 0\n\t" DMA_2("%2", "%3", "") DMA_2("%2", "%3", "") DMA_2("%2", "%3", "") DMA_2("%2", "%3", "") DMA_2("%2", "%3", "") DMA_2("%2", "%3", "") DMA_2("%2", "%3", "") DMA_2("%2", "%3", "")
                 : "+&s"(soff) : "s"(ldsaddr), "v"(voe), "v"(voo), "s"(rs), "s"(p1), "s"(d2) : "memory", "scc");
  else
    asm volatile("s_mov_b32 m0, %1\n\ts_nop 0\n\t" DMA_2("%2", "%2", "nt ") DMA_2("%3", "%3", "nt ") DMA_2("%2", "%2", "nt ") DMA_2("%3", "%3", "nt ") DMA_2("%2", "%2", "nt ") DMA_2("%3", "%3", "nt ") DMA_2("%2", "%2", "nt ") DMA_2("%3", "%3", "nt ")
                 : "+&s"(soff) : "s"(ldsaddr), "v"(voe), "v"(voo), "s"(rs), "s"(p1), "s"(d2) : "memory", "scc");
}
DI bf16x8 rd128(const LAS char* p) { return *(const LAS bf16x8*)p; }
DI bf16x8 rdtr(const LAS char* lo, const LAS char* up) {
  s16x4 a = __builtin_amdgcn_ds_read_tr16_b64_v4i16((LAS s16x4*)lo);
  s16x4 b = __builtin_amdgcn_ds_read_tr16_b64_v4i16((LAS s16x4*)up);
  s16x8 c = __builtin_shufflevector(a, b, 0, 1, 2, 3, 4, 5, 6, 7);
  return __builtin_bit_cast(bf16x8, c);
}
#define BAR() asm volatile("s_barrier" ::: "memory")
#define BAR_L() asm volatile("s_waitcnt lgkmcnt(0)\n\ts_barrier" ::: "memory")
#define WAITV(N) asm volatile("s_waitcnt vmcnt(" #N ")" ::: "memory")
#define WAITV_BAR(N) asm volatile("s_waitcnt vmcnt(" #N ")\n\ts_barrier" ::: "memory")
#define MX3(a, b, c) __builtin_fmaxf(__builtin_fmaxf((a), (b)), (c))
DI float both_max(float x) { auto r = __builtin_amdgcn_permlane32_swap(__float_as_uint(x), __float_as_uint(x), false, false); return __builtin_fmaxf(__uint_as_float(r[0]), __uint_as_float(r[1])); }
DI float both_sum(float x) { auto r = __builtin_amdgcn_permlane32_swap(__float_as_uint(x), __float_as_uint(x), false, false); return __uint_as_float(r[0]) + __uint_as_float(r[1]); }
DI void mfma_acc(f32x16& d, const bf16x8& a, const bf16x8& b) { asm volatile("v_mfma_f32_32x32x16_bf16 %0, %1, %2, %0" : "+v"(d) : "v"(a), "v"(b) : "memory"); }
DI void mfma_new(f32x16& d, const bf16x8& a, const bf16x8& b, const f32x16& c) { asm volatile("v_mfma_f32_32x32x16_bf16 %0, %1, %2, %3" : "=&v"(d) : "v"(a), "v"(b), "v"(c) : "memory"); }
DI float aexp(float x) { asm volatile("v_exp_f32 %0, %0" : "+v"(x) :: "memory"); return x; }
DI unsigned acvt(float a, float b) { unsigned r; asm volatile("v_cvt_pk_bf16_f32 %0, %1, %2" : "=v"(r) : "v"(a), "v"(b) : "memory"); return r; }
DI void aadd1(float& l, float a) { asm volatile("v_add_f32 %0, %0, %1" : "+v"(l) : "v"(a) : "memory"); }
DI void aadd2(float& l, float a, float b) { asm volatile("v_add_f32 %0, %0, %1\n\tv_add_f32 %0, %0, %2" : "+&v"(l) : "v"(a), "v"(b) : "memory"); }
DI void aadd3(float& l, float a, float b, float c) { asm volatile("v_add_f32 %0, %0, %1\n\tv_add_f32 %0, %0, %2\n\tv_add_f32 %0, %0, %3" : "+&v"(l) : "v"(a), "v"(b), "v"(c) : "memory"); }
DI void aadd4(float& l, float a, float b, float c, float d) { asm volatile("v_add_f32 %0, %0, %1\n\tv_add_f32 %0, %0, %2\n\tv_add_f32 %0, %0, %3\n\tv_add_f32 %0, %0, %4" : "+&v"(l) : "v"(a), "v"(b), "v"(c), "v"(d) : "memory"); }
DI float amax7(float a, float b, float c, float d, float e, float f, float g) { float r; asm volatile("v_max3_f32 %0, %1, %2, %3\n\tv_max3_f32 %0, %0, %4, %5\n\tv_max3_f32 %0, %0, %6, %7" : "=&v"(r) : "v"(a), "v"(b), "v"(c), "v"(d), "v"(e), "v"(f), "v"(g) : "memory"); return r; }
DI void amax6(float& r, float a, float b, float c, float d, float e, float f) { asm volatile("v_max3_f32 %0, %0, %1, %2\n\tv_max3_f32 %0, %0, %3, %4\n\tv_max3_f32 %0, %0, %5, %6" : "+&v"(r) : "v"(a), "v"(b), "v"(c), "v"(d), "v"(e), "v"(f) : "memory"); }
DI void amax8(float& r, float a, float b, float c, float d, float e, float f, float g, float h) { asm volatile("v_max3_f32 %0, %0, %1, %2\n\tv_max3_f32 %0, %0, %3, %4\n\tv_max3_f32 %0, %0, %5, %6\n\tv_max3_f32 %0, %0, %7, %8" : "+&v"(r) : "v"(a), "v"(b), "v"(c), "v"(d), "v"(e), "v"(f), "v"(g), "v"(h) : "memory"); }
DI void amax3z(float& r, float a, float b, float c) { asm volatile("v_max3_f32 %0, %0, %1, %2\n\tv_max3_f32 %0, %0, %3, 0" : "+&v"(r) : "v"(a), "v"(b), "v"(c) : "memory"); }
DI int nxt5(int s) { return s == 4 * SLOT ? 0 : s + SLOT; }
struct Tile { u32x4 v[16]; };
DI void tile_init(Tile& t) {
#pragma unroll
  for (int p = 0; p < 16; ++p) asm volatile("" : "=v"(t.v[p]));
}
template <int AUX, int IMM> DI void ld16(u32x4& d, __amdgpu_buffer_rsrc_t rs, unsigned voff, unsigned soff) {
  if constexpr (AUX == NT) asm volatile("buffer_load_dwordx4 %0, %1, %2, %3 offen offset:%4 nt" : "+v"(d) : "v"(voff), "s"(rs), "s"(soff), "i"(IMM) : "memory");
  else                     asm volatile("buffer_load_dwordx4 %0, %1, %2, %3 offen offset:%4" : "+v"(d) : "v"(voff), "s"(rs), "s"(soff), "i"(IMM) : "memory");
}
template <int P> DI void wr16(const u32x4& d, unsigned addr) { asm volatile("ds_write_b128 %0, %1 offset:%2" ::"v"(addr), "v"(d), "i"(P * 1024) : "memory"); }
DI void tile_wr(const Tile& t, unsigned addr) {
  wr16<0>(t.v[0], addr); wr16<1>(t.v[1], addr); wr16<2>(t.v[2], addr); wr16<3>(t.v[3], addr); wr16<4>(t.v[4], addr); wr16<5>(t.v[5], addr); wr16<6>(t.v[6], addr); wr16<7>(t.v[7], addr);
  wr16<8>(t.v[8], addr); wr16<9>(t.v[9], addr); wr16<10>(t.v[10], addr); wr16<11>(t.v[11], addr); wr16<12>(t.v[12], addr); wr16<13>(t.v[13], addr); wr16<14>(t.v[14], addr); wr16<15>(t.v[15], addr);
}

__global__ __launch_bounds__(512, 2) void mha_fused(const char* x, const char* kc, const char* vc, const char* wq, const char* wo, char* out, char* ws) {
  __shared__ __attribute__((aligned(16))) char lds[LDS_BYTES];
  u64 pc0; asm volatile("s_getpc_b64 %0" : "=s"(pc0));
  const int tid = threadIdx.x, lane = tid & 63;
  const int wid = rfl(tid >> 6);
  const int wg = blockIdx.x;
  const int xcd = wg & 7, j = wg >> 3;
  const int b = j & 7, h = xcd * 4 + (j >> 3);
  const bool late = (j ^ (j >> 3)) & 1;
#define PROJ_MAP() int wg_ = wg; asm volatile("" : "+s"(wg_)); \
  const int kh = wg_ & 1, kq = 2 * kh + (int)late; \
  const int ob = (wg_ & 4) + (wg_ >> 6), nb = (wg_ & 2) * 2 + ((wg_ >> 4) & 3); \
  const int ne = late ? 8 - UE : UE, ns = late ? 16 - UE - UL : UE + UL, e0 = late ? UE : 0, l0 = (late ? UL : 0) - ne; \
  const int hm0 = 16 * kh + 2 * e0 + (ob & 1), hm1 = 2 * (l0 - e0) + 1 - 2 * (ob & 1); \
  auto hmap = [&](int p) { return hm0 + 2 * p + (((ne - 1 - p) >> 31) & hm1); }
  const unsigned lds0 = (unsigned)(size_t)(LAS char*)lds;

  const __amdgpu_buffer_rsrc_t rcode = SRD(pc0 & ~255ull, CODE_BYTES);
  u32x4 cw; asm volatile("" : "=v"(cw));
  if (wid < 4) ld16<0, 0>(cw, rcode, lane * 16u, ((j & 7) * 4 + wid) * 1024u);
  asm volatile("" : "+s"(x), "+s"(kc), "+s"(vc), "+s"(wq), "+s"(wo), "+s"(out), "+s"(ws));
  GAS u64* flags = (GAS u64*)(ws + WS_FLAGS);

  if (__builtin_expect(wid >= 4, 1)) {
    const unsigned r8 = (lane >> 2) & 7, sub = lane >> 5, c4 = lane & 3;
    const unsigned hb = wid & 1;
    const unsigned xe = hb ? c4 : (c4 ^ (r8 >> 2)), xo = hb ? c4 : (c4 ^ (2 + (r8 >> 2)));
    const unsigned ldh = lds0 + hb * 16384u;
    if (__builtin_expect(wid >= 6, 0)) {
      const __amdgpu_buffer_rsrc_t rkv = SRD((hb ? vc : kc) + (size_t)(b * NH + h) * SK * DH * 2, SK * DH * 2);
      const unsigned kve = r8 * 256u + sub * 64u + 16u * xe, kvo = r8 * 256u + sub * 64u + 16u * xo;
      const int krot = (j + 5 * xcd) & 15;
      const unsigned wdst = ldh + lane * 16u;
      auto tile_ld = [&](Tile& R, int t) {
#pragma unroll
        for (int q = 0; q < 4; ++q) {
          const unsigned so = ((t + krot) & 15) * 16384u + q * 4096u;
          ld16<NT, 0>(R.v[4 * q], rkv, kve, so); ld16<NT, 128>(R.v[4 * q + 1], rkv, kve, so);
          ld16<NT, 2048>(R.v[4 * q + 2], rkv, kvo, so); ld16<NT, 2048 + 128>(R.v[4 * q + 3], rkv, kvo, so);
        }
      };
      auto tile_dma = [&](int t, int so) { dma_tile<1>(rkv, kve, kvo, ((t + krot) & 15) * 16384u, 128u, 2048u, ldh + so); };
      PROJ_MAP();
      const __amdgpu_buffer_rsrc_t rwo = SRD(wo + nb * 256, NH * DH * DM * 2 - nb * 256);
      const unsigned vw0 = (hb * 64u + r8) * 2048u + 16u * c4 + 64u * ((kq + sub) & 3), vw1 = (hb * 64u + r8) * 2048u + 16u * c4 + 64u * ((kq + 2 + sub) & 3);
      auto w_dma = [&](int p, int so) { dma_tile<0>(rwo, vw0, vw1, (unsigned)hmap(p) * (128u * 2048u), 0u, 16384u, ldh + so); };
      int sl = 3 * SLOT;
      int n0 = 5;
      if (late) {
        Tile R0, R1, R2;
        tile_init(R0); tile_init(R1); tile_init(R2);
#pragma unroll 1
        for (int c = -1; c <= KV_B; ++c) BAR();
        tile_dma(0, 3 * SLOT); tile_dma(1, 4 * SLOT); tile_ld(R0, 2);
        WAITV(32); tile_ld(R1, 3);
        WAITV(32); tile_ld(R2, 4);
#pragma unroll 1
        for (int c = KV_B + 1; c <= 14; ++c) BAR();
        WAITV_BAR(0);
        tile_wr(R0, wdst);
        BAR_L(); tile_wr(R1, wdst + SLOT);
        if (hb) BAR_L();
        tile_wr(R2, wdst + 2 * SLOT);
        asm volatile("s_waitcnt lgkmcnt(0)" ::: "memory");
      } else {
#pragma unroll 1
        for (int c = -1; c <= 12 + (int)hb; ++c) BAR();
        __builtin_amdgcn_s_sleep(6);
        tile_dma(0, sl); sl = nxt5(sl);
        n0 = 1;
      }
      if (late) {
        BAR(); BAR();
#pragma unroll 1
        for (int n = 5; n <= THIN_TO; ++n) { WAITV_BAR(0); tile_dma(n, sl); sl = nxt5(sl); }
        tile_dma(THIN_TO + 1, sl); sl = nxt5(sl);
        WAITV_BAR(16); tile_dma(THIN_TO + 2, sl); sl = nxt5(sl); tile_dma(THIN_TO + 3, sl); sl = nxt5(sl);
        n0 = THIN_TO + 4;
      }
#pragma unroll 1
      for (int n = n0; n <= 15; ++n) { WAITV_BAR(32); tile_dma(n, sl); sl = nxt5(sl); }
      if (!hb) {
        WAITV_BAR(32); WAITV_BAR(16); w_dma(0, sl); sl = nxt5(sl);
        WAITV_BAR(16); w_dma(1, sl); sl = nxt5(sl);
        BAR(); w_dma(2, sl); sl = nxt5(sl);
      } else {
#pragma unroll 1
        for (int s = 0; s <= 2; ++s) { WAITV_BAR(32); w_dma(s, sl); sl = nxt5(sl); }
      }
      WAITV_BAR(32);
      WAITV(31);
      __builtin_amdgcn_s_sleep(8);
      w_dma(3, sl);
      if (ns > 4) {
        sl = nxt5(sl); w_dma(4, sl);
#pragma unroll 1
        for (int p = 5; p < ns; ++p) { BAR(); WAITV_BAR(47); sl = nxt5(sl); w_dma(p, sl); }
        BAR(); WAITV_BAR(47);
      }
      BAR(); WAITV_BAR(32); BAR(); WAITV_BAR(16); BAR(); WAITV_BAR(0); BAR(); BAR();
      return;
    }
    u64 tag = 0;
    {
      const int qrot = (2 * (j & 7) + 4 * (j >> 3)) & 15;
      u64 qbase = hb ? (u64)wq : (u64)x; unsigned qoff = hb ? h * DH * 2 : b * SQ * DM * 2, qn = hb ? DM * NH * DH * 2 : (b + 1) * SQ * DM * 2;
      unsigned rsh = hb ? 13u : 11u, cstr = hb ? 64u * 8192u : 128u, p1 = hb ? 128u : 16384u, p2 = hb ? 65536u : 32768u;
      asm volatile("" : "+s"(qbase), "+s"(qoff), "+s"(qn), "+s"(rsh), "+s"(cstr), "+s"(p1), "+s"(p2));
      const __amdgpu_buffer_rsrc_t rs = SRD(qbase, qn);
      const unsigned voe = (r8 << rsh) + sub * 64u + 16u * xe, voo = (r8 << rsh) + sub * 64u + 16u * xo;
      int sl = 2 * SLOT;
      tag = __hip_atomic_load((GAS u64*)(ws + WS_EPOCH), RLX_AGENT);
#pragma unroll 1
      for (int c = 0; c < 16; ++c) {
        dma_tile<0>(rs, voe, voo, qoff + ((c + qrot) & 15) * cstr, p1, p2, ldh + sl); sl = sl == 2 * SLOT ? 0 : sl + SLOT;
        if (c == 2) { WAITV_BAR(32); }
        if (c >= 2) { WAITV_BAR(16); }
      }
      WAITV_BAR(0); BAR();
    }
    tag += EPOCH_INC;
    {
      PROJ_MAP(); (void)kq;
      u64 seen = 0;
      const GAS u64* myflag = flags + (((ob * 2 + kh) * 8 + nb) * 64 + lane);
      auto gate = [&](u64 need) {
        if (hb) return;
#pragma unroll 1
        for (unsigned spins = 0; (seen & need) != need && spins < SPIN_CAP; ++spins) {
          seen = __ballot(__hip_atomic_load(myflag, RLX_AGENT) == tag);
          if ((seen & need) != need) __builtin_amdgcn_s_sleep(1);
        }
      };
      auto hbit = [&](int p) { return 0x0001000100010001ull << (hmap(p) & 15); };
#pragma unroll 1
      for (int t = 0; t <= 10; ++t) { BAR(); }
      u32x4 d0; asm volatile("" : "=v"(d0));
      ld16<0, 0>(d0, rcode, lane * 16u, ((j >> 1) * 2 + hb) * 1024u);
      BAR(); BAR(); BAR(); WAITV_BAR(0);
      asm volatile("" ::"v"(d0));
      BAR();
      gate(hbit(0) | hbit(1)); BAR();
#pragma unroll 1
      for (int m = 0; m < ns; ++m) { if (m + 2 < ns) gate(hbit(m + 2)); BAR(); BAR(); }
    }
    if (wg == 0 && wid == 4) {
#pragma unroll 1
      for (unsigned spins = 0; spins < SPIN_CAP; ++spins) {
        bool ok = true;
#pragma unroll
        for (int i = 0; i < 16; ++i) ok &= __hip_atomic_load(flags + (i * 512 + lane), RLX_AGENT) == tag;
        if (__all(ok)) break;
        __builtin_amdgcn_s_sleep(2);
      }
      if (lane == 0) __hip_atomic_store((GAS u64*)(ws + WS_EPOCH), tag, RLX_AGENT);
    }
    return;
  }
  const u64 tag = __hip_atomic_load((GAS u64*)(ws + WS_EPOCH), RLX_AGENT) + EPOCH_INC;
  const int r = lane & 31, hh = lane >> 5, qb = wid;
  const int tq = (lane >> 2) & 3, tp = lane & 3, blk = (lane >> 4) & 1;
  const LAS char* L = (const LAS char*)lds;
  const int rowe = 64 * (r & 7) + 16 * (hh ^ ((r >> 2) & 3));
  const int kbase_e = 2048 * (r >> 3) + rowe, kbase_o = kbase_e ^ 32;
  const int tbase = hh * 256 + tq * 64 + blk * 32 + tp * 8;
  bf16x8 qf[8];
  bf16x8 kf[16], vf[16];
  f32x16 acc[4] = {};
#define KRD(i, SP) kf[i] = rd128((SP) + ((((i) >> 1) & 1) ? kbase_o : kbase_e) + 512 * ((i) >> 2) + 8192 * ((i) & 1))
  {
    const int xbase_e = 1024 * (4 * qb + (r >> 3)) + rowe, xbase_o = xbase_e ^ 32;
    const int pp = (tp == 1) ? 2 : (tp == 2) ? 1 : tp;
    const int wbase = 16384 + hh * 2048 + tq * 64 + blk * 32 + pp * 8;
    bf16x8 xf[4], wf[16];
    int sl = 2 * SLOT;
#define WRD(i, SP) wf[i] = rdtr((SP) + wbase + 4096 * ((i) >> 2) + 512 * ((i) & 3), (SP) + wbase + 4096 * ((i) >> 2) + 512 * ((i) & 3) + 256)
#define XRD(ks, SP) xf[ks] = rd128((SP) + (((ks) & 1) ? xbase_o : xbase_e) + 512 * ((ks) >> 1))
#define GQ(g, BODY) do { acc[(g) & 3] = MFMA(wf[g], xf[(g) >> 2], acc[(g) & 3]); BODY; SBAR(); } while (0)
    __builtin_amdgcn_s_waitcnt(0xC07F);
    asm volatile("s_waitcnt vmcnt(0)\n\ts_barrier" ::"v"(cw) : "memory");
    { const LAS char* S = L + sl; XRD(0, S); WRD(0, S); SBAR(); WRD(1, S); WRD(2, S); SBAR(); WRD(3, S); XRD(1, S); SBAR(); WRD(4, S); SBAR(); WRD(5, S); SBAR(); }
#define QCHUNK(LAST) do { const LAS char* S = L + sl; sl = sl == 2 * SLOT ? 0 : sl + SLOT; const LAS char* N = (LAST) ? L + 3 * SLOT : L + sl; SBAR(); \
      GQ(0, { WRD(6, S); XRD(2, S); }); GQ(1, { WRD(7, S); XRD(3, S); }); GQ(2, { WRD(8, S); }); GQ(3, { WRD(9, S); WRD(10, S); }); GQ(4, WRD(11, S)); GQ(5, { WRD(12, S); WRD(13, S); }); \
      GQ(6, { WRD(14, S); }); GQ(7, WRD(15, S)); GQ(8, {}); GQ(9, {}); GQ(10, {}); \
      BAR_L(); SBAR(); GQ(11, {}); \
      if (!(LAST)) { XRD(0, N); WRD(0, N); SBAR(); GQ(12, { WRD(1, N); WRD(2, N); }); GQ(13, { WRD(3, N); XRD(1, N); }); GQ(14, { WRD(4, N); }); GQ(15, WRD(5, N)); } \
      else { GQ(12, { KRD(0, N); KRD(1, N); }); GQ(13, { KRD(2, N); KRD(3, N); }); GQ(14, { KRD(4, N); KRD(5, N); }); GQ(15, { KRD(6, N); KRD(7, N); }); } } while (0)
#pragma unroll 1
    for (int c = 0; c < 15; ++c) QCHUNK(false);
    QCHUNK(true);
#pragma unroll
    for (int d = 0; d < 4; ++d)
#pragma unroll
      for (int i = 0; i < 16; ++i) acc[d][i] *= C2;
#pragma unroll
    for (int s = 0; s < 8; ++s) qf[s] = pack8(acc[s >> 1], s & 1);
  }
  {
    f32x16 o[4] = {};
    float l = 0.f;
    f32x16 sA0, sA1, sB0, sB1, negm;
    u32x4 pw[4];
    const f32x16 zero16 = {};
    int sk = 3 * SLOT;
    int sv = 3 * SLOT + 16384;
#define VRD(i, SP) vf[i] = rdtr((SP) + tbase + 4096 * ((i) >> 2) + 512 * ((i) & 3), (SP) + tbase + 4096 * ((i) >> 2) + 512 * ((i) & 3) + 2048)
#define EX(v) __builtin_amdgcn_exp2f(v)
#define PF(k) __builtin_bit_cast(bf16x8, pw[k])
#define LW(n) __builtin_amdgcn_s_waitcnt(0xC07F | ((n) << 8))
    {
      const LAS char* KP = L + sk;
#pragma unroll
      for (int i = 8; i < 16; ++i) KRD(i, KP);
      sA0 = MFMA(kf[0], qf[0], zero16); sA1 = MFMA(kf[1], qf[0], zero16);
#pragma unroll
      for (int s = 1; s < 8; ++s) { sA0 = MFMA(kf[2 * s], qf[s], sA0); sA1 = MFMA(kf[2 * s + 1], qf[s], sA1); }
      BAR_L();
      sk = nxt5(sk);
      const LAS char* KN = L + sk;
      KRD(0, KN); SBAR(); KRD(1, KN); SBAR(); KRD(2, KN); SBAR(); KRD(3, KN); SBAR();
      float a = MX3(sA0[0], sA0[1], sA1[0]); a = MX3(a, sA1[1], sA0[2]);
#pragma unroll
      for (int i = 3; i < 16; i += 2) a = MX3(a, sA0[i], (i + 1 < 16) ? sA0[i + 1] : sA0[i]);
#pragma unroll
      for (int i = 2; i < 16; i += 2) a = MX3(a, sA1[i], sA1[i + 1]);
      const float m = both_max(a);
#pragma unroll
      for (int i = 0; i < 16; ++i) { sA0[i] -= m; sA1[i] -= m; negm[i] = -m; }
#pragma unroll
      for (int i = 0; i < 13; ++i) sA0[i] = EX(sA0[i]);
    }
#define GA(i, X, BODY) do { if ((i) < 2) mfma_new(X, kf[i], qf[0], negm); else mfma_acc(X, kf[i], qf[(i) >> 1]); if ((i) + 4 < 16) { KRD(((i) + 4) & 15, KP_); } else { VRD((i) & 3, VP_); } BODY; } while (0)
#define E1(Y, e) do { Y[e] = aexp(Y[e]); } while (0)
#define E2(Y, e) do { Y[e] = aexp(Y[e]); Y[e + 1] = aexp(Y[e + 1]); } while (0)
#define S1(Y, e) aadd1(l, Y[e])
#define S2(Y, e) aadd2(l, Y[e], Y[e + 1])
#define S3(Y, e) aadd3(l, Y[e], Y[e + 1], Y[e + 2])
#define S4(Y, e) aadd4(l, Y[e], Y[e + 1], Y[e + 2], Y[e + 3])
#define PK(k, d, Y, e) do { pw[k][d] = acvt(Y[e], Y[e + 1]); } while (0)
#define PHASE_A(X0, X1, Y0, Y1) do { const LAS char* KP_ = L + sk; const LAS char* VP_ = L + sv; \
      LW(2); GA(0,  X0, { E2(Y0, 13); }); \
             GA(1,  X1, { PK(0, 0, Y0, 0); E1(Y0, 15); E1(Y1, 0); }); \
      LW(2); GA(2,  X0, { E2(Y1, 1); }); \
             GA(3,  X1, { PK(0, 1, Y0, 2); E2(Y1, 3); }); \
      LW(2); GA(4,  X0, { E2(Y1, 5); asm volatile("" ::"v"(negm));   }); \
             GA(5,  X1, { PK(0, 2, Y0, 4); E2(Y1, 7); }); \
      LW(2); GA(6,  X0, { E2(Y1, 9); }); \
             GA(7,  X1, { PK(0, 3, Y0, 6); E2(Y1, 11); }); \
      LW(2); GA(8,  X0, { E2(Y1, 13); }); \
             GA(9,  X1, { PK(1, 0, Y0, 8); S2(Y0, 0); E1(Y1, 15); }); \
      LW(2); GA(10, X0, { PK(1, 1, Y0, 10); S3(Y0, 2); }); \
             GA(11, X1, { PK(1, 2, Y0, 12); S4(Y0, 5); }); \
      LW(2); GA(12, X0, { PK(1, 3, Y0, 14); S2(Y0, 9); }); \
             GA(13, X1, { S4(Y0, 11); }); \
      LW(4); GA(14, X0, { aadd3(l, Y0[15], Y1[0], Y1[1]); }); \
             GA(15, X1, { S2(Y1, 2); }); \
      } while (0)
#define GB(i, NEXTK, BODY) do { mfma_acc(o[(i) & 3], vf[i], PF((i) >> 2)); if ((i) + 4 < 16) { VRD(((i) + 4) & 15, VP_); } else if (NEXTK) { KRD((i) & 3, KN_); } BODY; } while (0)
#define PHASE_B(X0, X1, Y1, NEXTK) do { const LAS char* VP_ = L + sv; const LAS char* KN_ = L + sk; float a_; float alpha_ = 1.f; bool resc_ = false; \
      LW(4); GB(0, NEXTK, { PK(2, 0, Y1, 0); S2(Y1, 4); }); \
             GB(1, NEXTK, { PK(2, 1, Y1, 2); PK(2, 2, Y1, 4); PK(2, 3, Y1, 6); S1(Y1, 6); }); \
      LW(4); GB(2, NEXTK, { a_ = amax7(X0[0], X0[1], X0[2], X0[3], X0[4], X0[5], X0[6]); }); \
             GB(3, NEXTK, { amax8(a_, X0[7], X0[8], X0[9], X0[10], X0[11], X0[12], X0[13], X0[14]); }); \
      LW(4); GB(4, NEXTK, { amax6(a_, X0[15], X1[0], X1[1], X1[2], X1[3], X1[4]); }); \
             GB(5, NEXTK, { amax8(a_, X1[5], X1[6], X1[7], X1[8], X1[9], X1[10], X1[11], X1[12]); }); \
      LW(4); GB(6, NEXTK, { amax3z(a_, X1[13], X1[14], X1[15]); S1(Y1, 7); }); \
             GB(7, NEXTK, { S2(Y1, 8); }); \
      if (__builtin_expect(__any(a_ > THR), 0)) { const float dl_ = both_max(a_); alpha_ = __builtin_amdgcn_exp2f(-dl_); resc_ = true; \
        _Pragma("unroll") for (int i_ = 0; i_ < 16; ++i_) { X0[i_] -= dl_; X1[i_] -= dl_; negm[i_] -= dl_; } } \
      LW(4); GB(8,  NEXTK, { PK(3, 0, Y1, 8); PK(3, 1, Y1, 10); S1(Y1, 10); }); \
             GB(9,  NEXTK, { PK(3, 2, Y1, 12); PK(3, 3, Y1, 14); E1(X0, 0); }); \
      LW(4); GB(10, NEXTK, { S1(Y1, 11); E1(X0, 1); }); \
             GB(11, NEXTK, { E2(X0, 2); }); \
      LW(4); GB(12, NEXTK, { S1(Y1, 12); E2(X0, 4); }); \
             GB(13, NEXTK, { S1(Y1, 13); E2(X0, 6); }); \
      if (NEXTK) LW(2); else LW(0); \
             GB(14, NEXTK, { S1(Y1, 14); E2(X0, 8); }); \
             GB(15, NEXTK, { S1(Y1, 15); E2(X0, 10); E1(X0, 12); }); \
      if (__builtin_expect(resc_, 0)) { asm volatile("s_nop 7\n\ts_nop 7" : "+v"(o[0]), "+v"(o[1]), "+v"(o[2]), "+v"(o[3]) :: "memory"); \
        l *= alpha_; _Pragma("unroll") for (int d_ = 0; d_ < 4; ++d_) _Pragma("unroll") for (int i_ = 0; i_ < 16; ++i_) o[d_][i_] *= alpha_; } \
      sv = sv == 4 * SLOT + 16384 ? 16384 : sv + SLOT; } while (0)
    asm volatile("s_nop 1" : "+v"(negm), "+v"(sA0), "+v"(sA1) :: "memory");
#pragma unroll 1
    for (int t = 1; t < 15; t += 2) {
      PHASE_A(sB0, sB1, sA0, sA1); BAR(); sk = nxt5(sk); PHASE_B(sB0, sB1, sA1, true);
      PHASE_A(sA0, sA1, sB0, sB1); BAR(); sk = nxt5(sk); PHASE_B(sA0, sA1, sB1, true);
    }
    PHASE_A(sB0, sB1, sA0, sA1); BAR(); PHASE_B(sB0, sB1, sA1, false);
    {
      const LAS char* VP_ = L + sv;
#pragma unroll
      for (int i = 0; i < 16; ++i) VRD(i, VP_);
#pragma unroll
      for (int i = 13; i < 16; ++i) sB0[i] = EX(sB0[i]);
      const bf16x8 p0 = pack8(sB0, 0), p1 = pack8(sB0, 1);
#pragma unroll
      for (int db = 0; db < 4; ++db) o[db] = MFMA(vf[db], p0, o[db]);
#pragma unroll
      for (int i = 0; i < 16; ++i) sB1[i] = EX(sB1[i]);
#pragma unroll
      for (int db = 0; db < 4; ++db) o[db] = MFMA(vf[4 + db], p1, o[db]);
      const bf16x8 p2 = pack8(sB1, 0), p3 = pack8(sB1, 1);
#pragma unroll
      for (int db = 0; db < 4; ++db) o[db] = MFMA(vf[8 + db], p2, o[db]);
#pragma unroll
      for (int i = 0; i < 16; ++i) l += sB0[i];
#pragma unroll
      for (int db = 0; db < 4; ++db) o[db] = MFMA(vf[12 + db], p3, o[db]);
#pragma unroll
      for (int i = 0; i < 16; ++i) l += sB1[i];
      l = both_sum(l);
    }
    const float inv = __builtin_amdgcn_rcpf(l);
    const __amdgpu_buffer_rsrc_t rs = SRD(ws + (unsigned)(((b * 4 + qb) * 256 + h * 8) * 1024), 8192);
#pragma unroll
    for (int db = 0; db < 4; ++db) {
#pragma unroll
      for (int i = 0; i < 16; ++i) o[db][i] *= inv;
#pragma unroll
      for (int s = 0; s < 2; ++s) __builtin_amdgcn_raw_buffer_store_b128(__builtin_bit_cast(u32x4, pack8(o[db], s)), rs, lane * 16 + (db * 2 + s) * 1024, 0, SC1);
    }
#pragma unroll
    for (int i = 0; i < 4; ++i) acc[i] = (f32x16){};
    asm volatile("s_waitcnt vmcnt(0)" : "+v"(acc[0]), "+v"(acc[1]), "+v"(acc[2]), "+v"(acc[3]) :: "memory");
    if (lane < 8) __hip_atomic_store(flags + (((b * 2 + (h >> 4)) * 8 + lane) * 64 + qb * 16 + (h & 15)), tag, RLX_AGENT);
  }
  {
    PROJ_MAP();
    BAR_L();
    const __amdgpu_buffer_rsrc_t rso = SRD(ws + (size_t)(ob * 4 + qb) * 262144, 262144);
    u32x4 bq[2][8];
    bf16x8 wf[16];
    int sw = 4 * SLOT;
#define OL1(lb, lh, so_, i_) bq[lb][4 * (lh) + (i_)] = __builtin_amdgcn_raw_buffer_load_b128(rso, lane * 16 + (i_) * 1024, so_, SC1)
#define OLD4(buf, p, hf) do { const unsigned so_ = (unsigned)hmap(p) * 8192u + (hf) * 4096u; _Pragma("unroll") for (int i_ = 0; i_ < 4; ++i_) { OL1(buf, hf, so_, i_); SBAR(); } } while (0)
#define ORD(i, SP) wf[i] = rdtr((SP) + tbase + 4096 * ((i) >> 2) + 512 * ((i) & 3), (SP) + tbase + 4096 * ((i) >> 2) + 512 * ((i) & 3) + 2048)
#define GO(buf, hf, g, BODY) do { acc[(g) & 3] = MFMA(wf[g], __builtin_bit_cast(bf16x8, bq[buf][4 * (hf) + ((g) >> 2)]), acc[(g) & 3]); BODY; SBAR(); } while (0)
#define OHALF(buf, hf, S, N, SYNC, MORE, LD, lb, lh, lp) do { unsigned so_ = 0; SBAR(); \
      GO(buf, hf, 0, { ORD(6, S); so_ = (unsigned)hmap(lp) * 8192u + (lh) * 4096u + (unsigned)(((ns - 1 - (lp)) >> 31) & 0x40000000); asm volatile("" : "+s"(so_)); }); GO(buf, hf, 1, { ORD(7, S); if (LD) OL1(lb, lh, so_, 0); }); \
      GO(buf, hf, 2, { ORD(8, S); if (LD) OL1(lb, lh, so_, 1); }); GO(buf, hf, 3, { ORD(9, S); if (LD) OL1(lb, lh, so_, 2); }); GO(buf, hf, 4, { ORD(10, S); if (LD) OL1(lb, lh, so_, 3); }); \
      GO(buf, hf, 5, { ORD(11, S); ORD(12, S); }); GO(buf, hf, 6, { ORD(13, S); ORD(14, S); }); GO(buf, hf, 7, ORD(15, S)); GO(buf, hf, 8, {}); GO(buf, hf, 9, {}); GO(buf, hf, 10, {}); \
      if ((SYNC) == 1) { BAR_L(); SBAR(); } else if ((SYNC) == 2) { BAR(); SBAR(); } \
      GO(buf, hf, 11, if (MORE) ORD(0, N)); GO(buf, hf, 12, if (MORE) { ORD(1, N); ORD(2, N); }); GO(buf, hf, 13, if (MORE) ORD(3, N)); GO(buf, hf, 14, if (MORE) ORD(4, N)); GO(buf, hf, 15, if (MORE) ORD(5, N)); } while (0)
#define OSTAGE(buf, p, LAST, LD2) do { const LAS char* S_ = L + sw; sw = nxt5(sw); const LAS char* N_ = L + sw; \
      OHALF(buf, 0, S_, S_ + 16384, (LAST) ? 0 : 2, true, !(LAST), (buf) ^ 1, 1, (p) + 1); \
      OHALF(buf, 1, S_ + 16384, N_, (LAST) ? 0 : 1, !(LAST), LD2, buf, 0, (p) + 2); } while (0)
    OLD4(0, 0, 0); OLD4(0, 0, 1); OLD4(1, 1, 0);
    { const LAS char* S_ = L + sw; ORD(0, S_); SBAR(); ORD(1, S_); ORD(2, S_); SBAR(); ORD(3, S_); SBAR(); ORD(4, S_); SBAR(); ORD(5, S_); SBAR(); }
#pragma unroll 1
    for (int p = 0; p < ns; p += 2) { OSTAGE(0, p, false, true); OSTAGE(1, p + 1, false, true); }
    const int tile = ob * 8 + nb;
    GAS u64* flags2 = (GAS u64*)(ws + WS_FLAGS2) + (tile * 16 + qb) * 16;
    {
      const __amdgpu_buffer_rsrc_t rp = SRD(ws + WS_PART + (size_t)tile * 131072 + (kq * 4 + qb) * 2048, 131072);
#pragma unroll
      for (int i = 1; i < 4; ++i)
#pragma unroll
        for (int g = 0; g < 2; ++g) __builtin_amdgcn_raw_buffer_store_b128(__builtin_bit_cast(u32x4, pack8(acc[i], g)), rp, lane * 16 + ((kq + i) & 3) * 32768u + g * 1024, 0, SC1);
      WAITV(0);
      if (lane < 4) __hip_atomic_store(flags2 + lane * 64 + kq, tag, RLX_AGENT);
    }
#pragma unroll 1
    for (unsigned spins = 0; spins < SPIN_CAP; ++spins) {
      if (__all(__hip_atomic_load(flags2 + kq * 64 + (lane & 3), RLX_AGENT) == tag || (lane & 3) == kq)) break;
      __builtin_amdgcn_s_sleep(1);
    }
    __builtin_amdgcn_fence(__ATOMIC_ACQUIRE, "wavefront");
    {
      const __amdgpu_buffer_rsrc_t rp = SRD(ws + WS_PART + (size_t)tile * 131072 + kq * 32768 + qb * 2048, 32768);
      u32x4 pv[3][2];
#pragma unroll
      for (int i = 1; i < 4; ++i)
#pragma unroll
        for (int g = 0; g < 2; ++g) pv[i - 1][g] = __builtin_amdgcn_raw_buffer_load_b128(rp, lane * 16 + g * 1024, ((kq + i) & 3) * 8192u, SC1);
      f32x16 sum = acc[0];
#pragma unroll
      for (int i = 0; i < 3; ++i)
#pragma unroll
        for (int e = 0; e < 8; ++e) { const unsigned w = pv[i][e >> 2][e & 3]; sum[2 * e] += __uint_as_float(w << 16); sum[2 * e + 1] += __uint_as_float(w & 0xffff0000u); }
      GAS char* orow = (GAS char*)out + (size_t)(ob * 128 + qb * 32 + r) * (DM * 2) + (nb * 128 + kq * 32) * 2 + hh * 16;
#pragma unroll
      for (int g = 0; g < 2; ++g) {
        unsigned a0 = cvtpk(sum[8 * g], sum[8 * g + 1]), a1 = cvtpk(sum[8 * g + 2], sum[8 * g + 3]);
        unsigned b0 = cvtpk(sum[8 * g + 4], sum[8 * g + 5]), b1 = cvtpk(sum[8 * g + 6], sum[8 * g + 7]);
        auto r0 = __builtin_amdgcn_permlane32_swap(a0, b0, false, false);
        auto r1 = __builtin_amdgcn_permlane32_swap(a1, b1, false, false);
        u32x4 w = {r0[0], r1[0], r0[1], r1[1]};
        *(GAS u32x4*)(orow + g * 32) = w;
      }
    }
  }
}

extern "C" void kernel_launch(void* const* d_in, const int* in_sizes, int n_in, void* d_out, int out_size, void* d_ws, size_t ws_size, hipStream_t stream) {
  mha_fused<<<256, 512, 0, stream>>>((const char*)d_in[0], (const char*)d_in[1], (const char*)d_in[2], (const char*)d_in[3], (const char*)d_in[4], (char*)d_out, (char*)d_ws);
}
```
